```python
import torch

def _softmax(x, axis=-1):
    return torch.softmax(x, dim=axis)
def _gelu(x):
    return torch.nn.functional.gelu(x, approximate="tanh")
def _silu(x):
    return torch.nn.functional.silu(x)
def _sigmoid(x):
    return torch.sigmoid(x)
def _layernorm(x, axis=-1, eps=1e-5):
    m = x.mean(dim=axis, keepdim=True)
    v = x.var(dim=axis, keepdim=True, unbiased=False)
    return (x - m) / torch.sqrt(v + eps)
def _scatter_add(tgt, idx, src, axis=0):
    return tgt.clone().index_add_(axis, idx, src)
def _segment_sum(src, seg, n):
    out = torch.zeros((n,) + src.shape[1:], dtype=src.dtype)
    return out.index_add_(0, seg, src)
def _pad(x, pw):
    flat = tuple(v for pair in reversed(pw) for v in pair)
    return torch.nn.functional.pad(x, flat)

kernel_name = 'standalone__gqa_softmax__decode'

def setup_inputs():

    _w = (lambda a: (lambda t: t.to(torch.bfloat16) if t.is_floating_point() else t)(torch.as_tensor(a)))
    B, Hq, Hkv, Sq, Sk, D = 4, 16, 8, 1, 16384, 256
    return {
        "q": _w((lambda *s: torch.randn(*s, dtype=torch.bfloat16) * 0.8)(B, Hq, Sq, D)),
        "k": _w((lambda *s: torch.randn(*s, dtype=torch.bfloat16) * 0.8)(B, Hkv, Sk, D)),
        "v": _w((lambda *s: torch.randn(*s, dtype=torch.bfloat16) * 0.05)(B, Hkv, Sk, D)),
    }

def reference(q, k, v):
    B, Hq, Sq, D = q.shape
    Hkv = k.shape[1]
    rep = Hq // Hkv
    k = (lambda x, n, axis: torch.repeat_interleave(x, n, dim=axis))(k, rep, axis=1)
    v = (lambda x, n, axis: torch.repeat_interleave(x, n, dim=axis))(v, rep, axis=1)
    scale = D ** -0.5
    scores = torch.einsum("bhqd,bhkd->bhqk", q, k) * scale
    p = _softmax(scores, axis=-1)
    return torch.einsum("bhqk,bhkd->bhqd", p, v)
```

```cpp
#include <hip/hip_runtime.h>
#include <hip/hip_bf16.h>
#include <stdint.h>

typedef __attribute__((ext_vector_type(4))) unsigned u32x4;
typedef __attribute__((ext_vector_type(4))) float f32x4;
typedef __attribute__((ext_vector_type(2))) float f32x2;
typedef __attribute__((ext_vector_type(2))) __bf16 bf16x2;

#define NWAVE 8
#define NSPLIT 8
#define ILV 1
#define KV_NT 1

constexpr int BATCH = 4, HQ = 16, HKV = 8, SK = 16384, D = 256;
constexpr int NGRP = BATCH * HKV;
constexpr int NTHR = NWAVE * 64;
constexpr int KB = 32;
constexpr int NBLK = SK / KB;
constexpr int NIT = NBLK / (NSPLIT * NWAVE);
static_assert(NIT * NSPLIT * NWAVE == NBLK, "even split");
constexpr float C2 = 0.0625f * 1.4426950408889634f;

__device__ __forceinline__ float dot2(unsigned k, unsigned q, float acc) {
  return __builtin_amdgcn_fdot2_f32_bf16(__builtin_bit_cast(bf16x2, k), __builtin_bit_cast(bf16x2, q), acc, false);
}
template <int CTRL> __device__ __forceinline__ float dpp(float x) {
  return __builtin_bit_cast(float, __builtin_amdgcn_mov_dpp(__builtin_bit_cast(int, x), CTRL, 0xf, 0xf, true));
}
constexpr int XOR1 = 0xB1, XOR2 = 0x4E, XOR7 = 0x141, XOR8 = 0x128;
__device__ __forceinline__ float xrow_max(float x) {
  auto s = __builtin_amdgcn_permlane16_swap(__float_as_uint(x), __float_as_uint(x), false, false);
  x = fmaxf(__uint_as_float(s[0]), __uint_as_float(s[1]));
  auto t = __builtin_amdgcn_permlane32_swap(__float_as_uint(x), __float_as_uint(x), false, false);
  return fmaxf(__uint_as_float(t[0]), __uint_as_float(t[1]));
}
__device__ __forceinline__ float xrow_sum(float x) {
  auto s = __builtin_amdgcn_permlane16_swap(__float_as_uint(x), __float_as_uint(x), false, false);
  x = __uint_as_float(s[0]) + __uint_as_float(s[1]);
  auto t = __builtin_amdgcn_permlane32_swap(__float_as_uint(x), __float_as_uint(x), false, false);
  return __uint_as_float(t[0]) + __uint_as_float(t[1]);
}
__device__ __forceinline__ float half_sum(float x) {
  auto t = __builtin_amdgcn_permlane32_swap(__float_as_uint(x), __float_as_uint(x), false, false);
  return __uint_as_float(t[0]) + __uint_as_float(t[1]);
}
__device__ __forceinline__ u32x4 ldkv(const u32x4* p) {
  return __builtin_nontemporal_load(p);
}

constexpr int REC = 2 * D + 4;

__global__ void __launch_bounds__(NTHR, NWAVE >= 16 ? 4 : (NWAVE / 4)) attn_split(const u32x4* __restrict__ q, const u32x4* __restrict__ k,
                                                    const u32x4* __restrict__ v, float* __restrict__ pml, float* __restrict__ po) {
  __shared__ __attribute__((aligned(16))) float lds[NWAVE * 64 + NWAVE * REC];
  const int tid = threadIdx.x, lane = tid & 63, wave = __builtin_amdgcn_readfirstlane(tid >> 6);
  const int grp = blockIdx.x / NSPLIT, split = blockIdx.x % NSPLIT;
  const int r = lane >> 3, c = lane & 7;

  u32x4 qf[2][4];
#pragma unroll
  for (int g = 0; g < 2; ++g)
#pragma unroll
    for (int i = 0; i < 4; ++i) qf[g][i] = q[(grp * 2 + g) * 32 + 8 * i + c];

  const size_t gbase = (size_t)grp * SK * 32;
  const int blk0 = split * NWAVE + wave;
  constexpr int BSTEP = NSPLIT * NWAVE;
  const u32x4* kp = k + gbase + (size_t)blk0 * 1024 + r * 32 + c;
  const u32x4* vp = v + gbase + (size_t)blk0 * 1024 + lane;

  u32x4 kb[16], vb[16];
#pragma unroll
  for (int j = 0; j < 16; ++j) kb[j] = ldkv(kp + (j >> 2) * 256 + (j & 3) * 8);
#pragma unroll
  for (int j = 0; j < 16; ++j) vb[j] = ldkv(vp + j * 64);
  __builtin_amdgcn_sched_barrier(0);

  float m = -INFINITY, l = 0.f;
  f32x2 acc[2][4];
#pragma unroll
  for (int g = 0; g < 2; ++g)
#pragma unroll
    for (int i = 0; i < 4; ++i) acc[g][i] = f32x2{0.f, 0.f};
  float* pbuf = lds + wave * 64;
  const int key_l = 8 * (c & 3) + r;
  const int pw = (key_l & 1) * 32 + (key_l >> 1) * 2 + (c >> 2);
  const int pr = (lane >> 5) * 32;

  auto body = [&](bool more) __attribute__((always_inline)) {
    kp += (size_t)BSTEP * 1024; vp += (size_t)BSTEP * 1024;
    float x[8];
#pragma unroll
    for (int u = 0; u < 4; ++u) {
      float d0 = 0.f, d1 = 0.f;
#pragma unroll
      for (int i = 0; i < 4; ++i) {
        const u32x4 kk = kb[u * 4 + i];
#pragma unroll
        for (int e = 0; e < 4; ++e) { d0 = dot2(kk[e], qf[0][i][e], d0); d1 = dot2(kk[e], qf[1][i][e], d1); }
        __builtin_amdgcn_sched_barrier(0);
        if (more) kb[u * 4 + i] = ldkv(kp + u * 256 + i * 8);
        __builtin_amdgcn_sched_barrier(0);
      }
      x[u] = d0; x[4 + u] = d1;
    }
#pragma unroll
    for (int h = 0; h < 4; ++h) { const float lo = x[h], hi = x[h + 4]; const float t1 = lo + dpp<XOR7>(lo), t2 = hi + dpp<XOR7>(hi); x[h] = (c & 4) ? t2 : t1; }
#pragma unroll
    for (int h = 0; h < 2; ++h) { const float lo = x[h], hi = x[h + 2]; const float t1 = lo + dpp<XOR2>(lo), t2 = hi + dpp<XOR2>(hi); x[h] = (c & 2) ? t2 : t1; }
    { const float lo = x[0], hi = x[1]; const float t1 = lo + dpp<XOR1>(lo), t2 = hi + dpp<XOR1>(hi); x[0] = (c & 1) ? t2 : t1; }
    const float s = x[0] * C2;
    float bm = fmaxf(s, dpp<XOR1>(s)); bm = fmaxf(bm, dpp<XOR2>(bm)); bm = fmaxf(bm, dpp<XOR8>(bm)); bm = xrow_max(bm);
    const float mn = fmaxf(m, bm);
    const float msub = (mn == -INFINITY) ? 0.f : mn;
    const float alpha = __builtin_amdgcn_exp2f(m - msub);
    const float p = __builtin_amdgcn_exp2f(s - msub);
    l = fmaf(l, alpha, p); m = mn;
    pbuf[pw] = p;
    const float a0 = __builtin_bit_cast(float, __builtin_amdgcn_readlane(__builtin_bit_cast(int, alpha), 0));
    const float a1 = __builtin_bit_cast(float, __builtin_amdgcn_readlane(__builtin_bit_cast(int, alpha), 4));
#pragma unroll
    for (int i = 0; i < 4; ++i) { acc[0][i] *= a0; acc[1][i] *= a1; }
    f32x4 pp[8];
#pragma unroll
    for (int jj = 0; jj < 8; ++jj) pp[jj] = *reinterpret_cast<const f32x4*>(pbuf + pr + 4 * jj);
    __builtin_amdgcn_sched_barrier(0);
#pragma unroll
    for (int j = 0; j < 16; ++j) {
      const u32x4 vv = vb[j];
      const float p0 = pp[j >> 1][(j & 1) * 2], p1 = pp[j >> 1][(j & 1) * 2 + 1];
#pragma unroll
      for (int e = 0; e < 4; ++e) {
        const f32x2 vf = f32x2{__builtin_bit_cast(float, vv[e] << 16), __builtin_bit_cast(float, vv[e] & 0xffff0000u)};
        acc[0][e] = __builtin_elementwise_fma(vf, f32x2{p0, p0}, acc[0][e]);
        acc[1][e] = __builtin_elementwise_fma(vf, f32x2{p1, p1}, acc[1][e]);
      }
      __builtin_amdgcn_sched_barrier(0);
      if (more) vb[j] = ldkv(vp + j * 64);
      __builtin_amdgcn_sched_barrier(0);
    }
  };
  for (int it = 0; it < NIT - 1; ++it) body(true);
  body(false);

  float* rec = lds + NWAVE * 64 + wave * REC;
  {
    float t = l; t += dpp<XOR1>(t); t += dpp<XOR2>(t); t += dpp<XOR8>(t); t = xrow_sum(t);
    if (lane == 0 || lane == 4) { rec[2 * D + (lane >> 2)] = m; rec[2 * D + 2 + (lane >> 2)] = t; }
    float o[8];
#pragma unroll
    for (int i = 0; i < 4; ++i)
#pragma unroll
      for (int e = 0; e < 2; ++e) {
        auto sw = __builtin_amdgcn_permlane32_swap(__float_as_uint(acc[0][i][e]), __float_as_uint(acc[1][i][e]), false, false);
        o[2 * i + e] = __uint_as_float(sw[0]) + __uint_as_float(sw[1]);
      }
    float* dst = rec + (lane >> 5) * D + (lane & 31) * 8;
    *reinterpret_cast<f32x4*>(dst) = f32x4{o[0], o[1], o[2], o[3]};
    *reinterpret_cast<f32x4*>(dst + 4) = f32x4{o[4], o[5], o[6], o[7]};
  }
  __syncthreads();
  for (int t = tid; t < 2 * D; t += NTHR) {
    const int g = t >> 8;
    const float* recs = lds + NWAVE * 64;
    float M = -INFINITY;
#pragma unroll
    for (int w = 0; w < NWAVE; ++w) M = fmaxf(M, recs[w * REC + 2 * D + g]);
    const float Ms = (M == -INFINITY) ? 0.f : M;
    float L = 0.f, o = 0.f;
#pragma unroll
    for (int w = 0; w < NWAVE; ++w) {
      const float wt = __builtin_amdgcn_exp2f(recs[w * REC + 2 * D + g] - Ms);
      L = fmaf(wt, recs[w * REC + 2 * D + 2 + g], L);
      o = fmaf(wt, recs[w * REC + t], o);
    }
    po[(size_t)blockIdx.x * 2 * D + t] = o;
    if ((t & 255) == 0) { pml[blockIdx.x * 4 + g * 2] = M; pml[blockIdx.x * 4 + g * 2 + 1] = L; }
  }
}

__global__ void __launch_bounds__(256) attn_combine(const float* __restrict__ pml, const float* __restrict__ po, __hip_bfloat16* __restrict__ out) {
  const int row = blockIdx.x, grp = row >> 1, g = row & 1, d = threadIdx.x;
  float ms[NSPLIT], ls[NSPLIT], os[NSPLIT];
#pragma unroll
  for (int s = 0; s < NSPLIT; ++s) {
    const f32x2 ml = *reinterpret_cast<const f32x2*>(pml + (grp * NSPLIT + s) * 4 + g * 2);
    ms[s] = ml[0]; ls[s] = ml[1];
    os[s] = po[(size_t)(grp * NSPLIT + s) * 2 * D + g * D + d];
  }
  float M = -INFINITY;
#pragma unroll
  for (int s = 0; s < NSPLIT; ++s) M = fmaxf(M, ms[s]);
  const float Ms = (M == -INFINITY) ? 0.f : M;
  float L = 0.f, o = 0.f;
#pragma unroll
  for (int s = 0; s < NSPLIT; ++s) { const float wt = __builtin_amdgcn_exp2f(ms[s] - Ms); L = fmaf(wt, ls[s], L); o = fmaf(wt, os[s], o); }
  out[row * D + d] = __float2bfloat16(o / L);
}

extern "C" void kernel_launch(void* const* d_in, const int* in_sizes, int n_in, void* d_out, int out_size, void* d_ws, size_t ws_size,
                              hipStream_t stream) {
  const u32x4* q = (const u32x4*)d_in[0];
  const u32x4* k = (const u32x4*)d_in[1];
  const u32x4* v = (const u32x4*)d_in[2];
  float* pml = (float*)d_ws;
  float* po = (float*)((char*)d_ws + 65536);
  attn_split<<<NGRP * NSPLIT, NTHR, 0, stream>>>(q, k, v, pml, po);
  attn_combine<<<BATCH * HQ, 256, 0, stream>>>(pml, po, (__hip_bfloat16*)d_out);
}
```

```cpp
#include <hip/hip_runtime.h>
#include <hip/hip_bf16.h>
#include <stdint.h>

typedef __attribute__((ext_vector_type(4))) unsigned u32x4;
typedef __attribute__((ext_vector_type(4))) float f32x4;
typedef __attribute__((ext_vector_type(2))) float f32x2;
typedef __attribute__((ext_vector_type(2))) double f64x2;
typedef __attribute__((ext_vector_type(2))) unsigned u32x2;
typedef __attribute__((ext_vector_type(2))) __bf16 bf16x2;
typedef __attribute__((address_space(1))) unsigned long long gu64;

#define CFG_U 1
#define CFG_NS 20
constexpr int U = CFG_U;
constexpr int NS = CFG_NS;

constexpr int BATCH = 4, HKV = 8, SK = 16384, D = 256;
constexpr int NGRP = BATCH * HKV;
constexpr int NWAVE = 8, NSPLIT = 8, NTHR = NWAVE * 64;
constexpr int UBYTES = 8 * U * D * 2;
constexpr int RUN = SK / (8 * U * NWAVE * NSPLIT);
constexpr int NDYN = (RUN - NS) * NWAVE * NSPLIT;
constexpr int SLOTB = 8192 + 128;
constexpr int GRPWS = NSPLIT * SLOTB;
constexpr int CTRL = 256;
static_assert(NS >= 2 && NDYN >= 0 && NDYN + 3 * NWAVE * NSPLIT < 65535, "unit split");
constexpr float C2 = 0.0625f * 1.4426950408889634f;
constexpr int RSTR = 258;
constexpr unsigned OOB = 0x40000000u;

__device__ __forceinline__ float dot2(unsigned k, unsigned q, float acc) {
  return __builtin_amdgcn_fdot2_f32_bf16(__builtin_bit_cast(bf16x2, k), __builtin_bit_cast(bf16x2, q), acc, false);
}
template <int CTRLW> __device__ __forceinline__ float dpp(float x) {
  return __builtin_bit_cast(float, __builtin_amdgcn_mov_dpp(__builtin_bit_cast(int, x), CTRLW, 0xf, 0xf, true));
}
template <int CTRLW> __device__ __forceinline__ double dpp(double x) {
  const u32x2 b = __builtin_bit_cast(u32x2, x);
  return __builtin_bit_cast(double, u32x2{(unsigned)__builtin_amdgcn_mov_dpp((int)b[0], CTRLW, 0xf, 0xf, true),
                                          (unsigned)__builtin_amdgcn_mov_dpp((int)b[1], CTRLW, 0xf, 0xf, true)});
}
constexpr int XOR1 = 0xB1, XOR2 = 0x4E, XOR7 = 0x141, XOR8 = 0x128, BCAST = 0x150;
__device__ __forceinline__ float xrow_max(float x) {
  auto s = __builtin_amdgcn_permlane16_swap(__float_as_uint(x), __float_as_uint(x), false, false);
  x = fmaxf(__uint_as_float(s[0]), __uint_as_float(s[1]));
  auto t = __builtin_amdgcn_permlane32_swap(__float_as_uint(x), __float_as_uint(x), false, false);
  return fmaxf(__uint_as_float(t[0]), __uint_as_float(t[1]));
}
__device__ __forceinline__ double swap16_sum(double a) {
  const u32x2 x = __builtin_bit_cast(u32x2, a);
  auto lo = __builtin_amdgcn_permlane16_swap(x[0], x[0], false, false);
  auto hi = __builtin_amdgcn_permlane16_swap(x[1], x[1], false, false);
  return __builtin_bit_cast(double, u32x2{lo[0], hi[0]}) + __builtin_bit_cast(double, u32x2{lo[1], hi[1]});
}
__device__ __forceinline__ double swap32_sum(double a, double b) {
  const u32x2 x = __builtin_bit_cast(u32x2, a), y = __builtin_bit_cast(u32x2, b);
  auto lo = __builtin_amdgcn_permlane32_swap(x[0], y[0], false, false);
  auto hi = __builtin_amdgcn_permlane32_swap(x[1], y[1], false, false);
  return __builtin_bit_cast(double, u32x2{lo[0], hi[0]}) + __builtin_bit_cast(double, u32x2{lo[1], hi[1]});
}
__device__ __forceinline__ float rdlane(float x, int l) {
  return __builtin_bit_cast(float, __builtin_amdgcn_readlane(__builtin_bit_cast(int, x), l));
}
__device__ __forceinline__ double rdlane(double x, int l) {
  const u32x2 b = __builtin_bit_cast(u32x2, x);
  return __builtin_bit_cast(double, u32x2{(unsigned)__builtin_amdgcn_readlane((int)b[0], l), (unsigned)__builtin_amdgcn_readlane((int)b[1], l)});
}
__device__ __forceinline__ int expo(float d) { return (int)fmaxf(d, -4000.f); }
__device__ __forceinline__ double pow2d(float d) { return __builtin_amdgcn_ldexp(1.0, expo(d)); }
__device__ __forceinline__ float pow2f(float d) { return __builtin_amdgcn_ldexpf(1.f, expo(d)); }
__device__ __forceinline__ u32x4 granules(double x, unsigned tag) {
  const u32x2 b = __builtin_bit_cast(u32x2, x);
  return u32x4{b[0], tag, b[1], tag};
}
__device__ __forceinline__ void lds_barrier() {
  asm volatile("s_waitcnt lgkmcnt(0)" ::: "memory");
  __builtin_amdgcn_s_barrier();
  asm volatile("" ::: "memory");
}
#define PIN() __builtin_amdgcn_sched_barrier(0)
#define RLX_AGENT __ATOMIC_RELAXED, __HIP_MEMORY_SCOPE_AGENT

__global__ void __launch_bounds__(NTHR, 2) attn_decode(const u32x4* __restrict__ q, const char* __restrict__ k,
                                                       const char* __restrict__ v, char* __restrict__ ws,
                                                       unsigned short* __restrict__ out) {
  __shared__ __attribute__((aligned(16))) double rec[NWAVE][2][RSTR];
  __shared__ __attribute__((aligned(16))) float mrec[2][NWAVE];
  const int tid = threadIdx.x, lane = tid & 63, wave = __builtin_amdgcn_readfirstlane(tid >> 6);
  const int grp = blockIdx.x / NSPLIT, split = blockIdx.x % NSPLIT;
  const int r = lane >> 3, c = lane & 7;
  gu64* const wq = (gu64*)(ws + grp * CTRL);
  gu64* const we = (gu64*)(ws + grp * CTRL + 128);
  const unsigned long long e_in = __hip_atomic_load(we, RLX_AGENT), w_in = __hip_atomic_load(wq, RLX_AGENT);
  PIN();
  unsigned zero = 0;
  asm volatile("" : "+v"(zero));
  gu64* const wql = wq + zero;

  const char* kg = k + (size_t)grp * SK * D * 2;
  const char* vg = v + (size_t)grp * SK * D * 2;
  const unsigned kvo = r * 512 + c * 16, vvo = (lane >> 5) * 2048 + (lane & 31) * 16;
  u32x4 kb[4 * U], vb[4 * U], qf[2][4];
  int nxt = split * (RUN * NWAVE) + wave;
  {
    const auto rk = __builtin_amdgcn_make_buffer_rsrc((void*)kg, 0, SK * D * 2, 0x00020000);
    const auto rv = __builtin_amdgcn_make_buffer_rsrc((void*)vg, 0, SK * D * 2, 0x00020000);
    const int so = nxt * UBYTES;
#pragma unroll
    for (int g = 0; g < 2; ++g)
#pragma unroll
      for (int i = 0; i < 4; ++i) { qf[g][i] = q[(grp * 2 + g) * 32 + 8 * i + c]; PIN(); }
#pragma unroll
    for (int j = 0; j < 4 * U; ++j) { kb[j] = __builtin_amdgcn_raw_buffer_load_b128(rk, kvo + (j & 3) * 128, so + (j >> 2) * 4096, 2); PIN(); }
#pragma unroll
    for (int j = 0; j < 4 * U; ++j) { vb[j] = __builtin_amdgcn_raw_buffer_load_b128(rv, vvo + (j & 3) * 512, so + (j >> 2) * 4096, 2); PIN(); }
  }
  const unsigned long long epoch = (e_in + 1) & 0xffffffffffffull;
  if (lane == 0 && (w_in >> 16) != epoch) {
    unsigned long long expect = w_in;
    __hip_atomic_compare_exchange_strong(wql, &expect, epoch << 16, __ATOMIC_RELAXED, __ATOMIC_RELAXED, __HIP_MEMORY_SCOPE_AGENT);
  }
  PIN();
  unsigned long long got = 0;

  float m = -INFINITY, l = 0.f, M = -INFINITY;
  f32x2 acc[2][4];
  double L = 0.0, A[2][8];
#pragma unroll
  for (int g = 0; g < 2; ++g)
#pragma unroll
    for (int i = 0; i < 4; ++i) { acc[g][i] = f32x2{0.f, 0.f}; A[g][2 * i] = 0.0; A[g][2 * i + 1] = 0.0; }

  for (int t = 0;; ++t) {
    if (t >= NS - 1) {
      asm volatile("" :: "v"(got));
      const int cq = __builtin_amdgcn_readfirstlane((unsigned)got) & 0xffff;
      nxt = cq < NDYN ? ((cq >> 3) & 7) * (RUN * NWAVE) + (NS + (cq >> 6)) * NWAVE + (cq & 7) : -1;
    } else nxt += NWAVE;
    if (t >= NS - 2 && nxt >= 0 && lane == 0) got = __hip_atomic_fetch_add(wql, 1ull, RLX_AGENT);
    PIN();
    const int so = (nxt >= 0 ? nxt : 0) * UBYTES, nrec = nxt >= 0 ? SK * D * 2 : 0;
    const auto rk = __builtin_amdgcn_make_buffer_rsrc((void*)kg, 0, nrec, 0x00020000);
    const auto rv = __builtin_amdgcn_make_buffer_rsrc((void*)vg, 0, nrec, 0x00020000);
    float x[U];
#pragma unroll
    for (int u = 0; u < U; ++u) {
      float d0 = 0.f, d1 = 0.f;
#pragma unroll
      for (int i = 0; i < 4; ++i) {
        const u32x4 kk = kb[u * 4 + i];
#pragma unroll
        for (int e = 0; e < 4; ++e) { d0 = dot2(kk[e], qf[0][i][e], d0); d1 = dot2(kk[e], qf[1][i][e], d1); }
        PIN();
        kb[u * 4 + i] = __builtin_amdgcn_raw_buffer_load_b128(rk, kvo + i * 128, so + u * 4096, 2);
        PIN();
      }
      const float t1 = d0 + dpp<XOR7>(d0), t2 = d1 + dpp<XOR7>(d1);
      x[u] = (c & 4) ? t2 : t1;
    }
    float s;
    if (U == 2) { const float t1 = x[0] + dpp<XOR2>(x[0]), t2 = x[U - 1] + dpp<XOR2>(x[U - 1]); s = (c & 2) ? t2 : t1; }
    else s = x[0] + dpp<XOR2>(x[0]);
    s = (s + dpp<XOR1>(s)) * C2;
    float bm = s;
    if (U == 2) bm = fmaxf(bm, dpp<XOR2>(bm));
    bm = fmaxf(bm, dpp<XOR8>(bm)); bm = xrow_max(bm);
    const float mn = fmaxf(m, __builtin_ceilf(bm));
    const float msub = (mn == -INFINITY) ? 0.f : mn;
    const float alpha = __builtin_amdgcn_exp2f(m - msub);
    const float p = __builtin_amdgcn_exp2f(s - msub);
    l = fmaf(l, alpha, p); m = mn;
    const float a0 = rdlane(alpha, 0), a1 = rdlane(alpha, 4);
#pragma unroll
    for (int i = 0; i < 4; ++i) { acc[0][i] *= a0; acc[1][i] *= a1; }
    const auto pr = __builtin_amdgcn_permlane16_swap(__float_as_uint(p), __float_as_uint(p), false, false);
    const float pa = __uint_as_float(pr[0]), pb = __uint_as_float(pr[1]);
    auto pv = [&](int j, float p0, float p1) __attribute__((always_inline)) {
      const u32x4 vv = vb[j];
#pragma unroll
      for (int e = 0; e < 4; ++e) {
        const f32x2 vf = f32x2{__builtin_bit_cast(float, vv[e] << 16), __builtin_bit_cast(float, vv[e] & 0xffff0000u)};
        acc[0][e] = __builtin_elementwise_fma(vf, f32x2{p0, p0}, acc[0][e]);
        acc[1][e] = __builtin_elementwise_fma(vf, f32x2{p1, p1}, acc[1][e]);
      }
      PIN();
      vb[j] = __builtin_amdgcn_raw_buffer_load_b128(rv, vvo + (j & 3) * 512, so + (j >> 2) * 4096, 2);
      PIN();
    };
    pv(0, dpp<BCAST + 0>(pa), dpp<BCAST + 4>(pa));
    pv(1, dpp<BCAST + 8>(pa), dpp<BCAST + 12>(pa));
    pv(2, dpp<BCAST + 0>(pb), dpp<BCAST + 4>(pb));
    pv(3, dpp<BCAST + 8>(pb), dpp<BCAST + 12>(pb));
    if (U == 2) {
      pv(4 * (U - 1) + 0, dpp<BCAST + 2>(pa), dpp<BCAST + 6>(pa));
      pv(4 * (U - 1) + 1, dpp<BCAST + 10>(pa), dpp<BCAST + 14>(pa));
      pv(4 * (U - 1) + 2, dpp<BCAST + 2>(pb), dpp<BCAST + 6>(pb));
      pv(4 * (U - 1) + 3, dpp<BCAST + 10>(pb), dpp<BCAST + 14>(pb));
    }
    if (t >= NS - 1) {
      const float Mn = fmaxf(M, m);
      const float Ms = (Mn == -INFINITY) ? 0.f : Mn;
      const double wa = pow2d(M - Ms);
      const float wb = pow2f(m - Ms);
      M = Mn;
      L = __builtin_fma(L, wa, (double)(l * wb));
#pragma unroll
      for (int g = 0; g < 2; ++g) {
        const double wag = rdlane(wa, 4 * g);
        const float wbg = rdlane(wb, 4 * g);
#pragma unroll
        for (int i = 0; i < 4; ++i) {
          const f32x2 y = acc[g][i] * wbg;
          A[g][2 * i] = __builtin_fma(A[g][2 * i], wag, (double)y[0]);
          A[g][2 * i + 1] = __builtin_fma(A[g][2 * i + 1], wag, (double)y[1]);
          acc[g][i] = f32x2{0.f, 0.f};
        }
      }
      m = -INFINITY; l = 0.f;
      if (nxt < 0) break;
    }
  }

  {
    double tl = L;
    if (U == 2) tl += dpp<XOR2>(tl);
    tl += dpp<XOR8>(tl); tl = swap16_sum(tl); tl = swap32_sum(tl, tl);
    if (lane == 0 || lane == 4) { mrec[lane >> 2][wave] = M; rec[wave][lane >> 2][D] = tl; }
    double* dst = &rec[wave][lane >> 5][(lane & 31) * 8];
#pragma unroll
    for (int i = 0; i < 4; ++i)
      *reinterpret_cast<f64x2*>(dst + 2 * i) = f64x2{swap32_sum(A[0][2 * i], A[1][2 * i]), swap32_sum(A[0][2 * i + 1], A[1][2 * i + 1])};
  }
  lds_barrier();
  const unsigned tag = (unsigned)epoch;
  const auto rw = __builtin_amdgcn_make_buffer_rsrc((void*)(ws + NGRP * CTRL + (size_t)grp * GRPWS), 0, GRPWS, 0x00020000);
  {
    const int g = wave >> 2;
    const f32x4 ma = *reinterpret_cast<const f32x4*>(&mrec[g][0]), mb = *reinterpret_cast<const f32x4*>(&mrec[g][4]);
    double ov[NWAVE], lv[NWAVE];
#pragma unroll
    for (int w = 0; w < NWAVE; ++w) { ov[w] = rec[w][g][tid & 255]; lv[w] = rec[w][g][D]; }
    const float Mw = fmaxf(fmaxf(fmaxf(ma[0], ma[1]), fmaxf(ma[2], ma[3])), fmaxf(fmaxf(mb[0], mb[1]), fmaxf(mb[2], mb[3])));
    const float Ms = (Mw == -INFINITY) ? 0.f : Mw;
    double a = 0.0, ls = 0.0;
#pragma unroll
    for (int w = 0; w < NWAVE; ++w) {
      const double wt = pow2d((w < 4 ? ma[w & 3] : mb[w & 3]) - Ms);
      a = __builtin_fma(ov[w], wt, a); ls = __builtin_fma(lv[w], wt, ls);
    }
    __builtin_amdgcn_raw_buffer_store_b128(granules(a, tag), rw, split * SLOTB + tid * 16, 0, 16);
    const u32x4 st = lane ? u32x4{__float_as_uint(Mw), tag, 0u, tag} : granules(ls, tag);
    __builtin_amdgcn_raw_buffer_store_b128(st, rw, ((wave & 3) == 0 && lane < 2) ? split * SLOTB + 8192 + g * 32 + lane * 16 : OOB, 0, 16);
  }
  if (wave != 0) return;

  u32x2 gv[NSPLIT + 1];
  bool ok[NSPLIT + 1];
#pragma unroll
  for (int i = 0; i <= NSPLIT; ++i) { ok[i] = i == NSPLIT && lane >= 16; gv[i] = u32x2{0, 0}; }
  for (int spin = 0; spin < (1 << 18); ++spin) {
    u32x4 nv[NSPLIT + 1];
#pragma unroll
    for (int i = 0; i <= NSPLIT; ++i) {
      const unsigned off = i < NSPLIT ? i * SLOTB + split * 1024 + lane * 16 : (lane >> 1) * SLOTB + 8192 + (split >> 2) * 32 + (lane & 1) * 16;
      nv[i] = __builtin_amdgcn_raw_buffer_load_b128(rw, ok[i] ? OOB : off, 0, 16);
    }
    PIN();
    bool all = true;
#pragma unroll
    for (int i = 0; i <= NSPLIT; ++i) {
      const bool fresh = (nv[i][1] == tag) & (nv[i][3] == tag) & !ok[i];
      gv[i][0] = fresh ? nv[i][0] : gv[i][0];
      gv[i][1] = fresh ? nv[i][2] : gv[i][1];
      ok[i] |= fresh;
      all &= ok[i];
    }
    if (__all(all)) break;
    __builtin_amdgcn_s_sleep(1);
    asm volatile("" ::: "memory");
  }
  {
    float ms[NSPLIT], Mx = -INFINITY;
#pragma unroll
    for (int i = 0; i < NSPLIT; ++i) { ms[i] = rdlane(__uint_as_float(gv[NSPLIT][0]), 2 * i + 1); Mx = fmaxf(Mx, ms[i]); }
    const float Ms = (Mx == -INFINITY) ? 0.f : Mx;
    double a = 0.0, ls = 0.0;
#pragma unroll
    for (int i = 0; i < NSPLIT; ++i) {
      const double wt = pow2d(ms[i] - Ms);
      a = __builtin_fma(__builtin_bit_cast(double, gv[i]), wt, a);
      ls = __builtin_fma(rdlane(__builtin_bit_cast(double, gv[NSPLIT]), 2 * i), wt, ls);
    }
    const __bf16 res = (__bf16)((float)a / (float)ls);
    out[(grp * 2 + (split >> 2)) * D + (split & 3) * 64 + lane] = __builtin_bit_cast(unsigned short, res);
    if (split == 0 && lane == 0) __hip_atomic_store(we, epoch, RLX_AGENT);
  }
}

extern "C" void kernel_launch(void* const* d_in, const int* in_sizes, int n_in, void* d_out, int out_size, void* d_ws, size_t ws_size,
                              hipStream_t stream) {
  attn_decode<<<NGRP * NSPLIT, NTHR, 0, stream>>>((const u32x4*)d_in[0], (const char*)d_in[1], (const char*)d_in[2], (char*)d_ws,
                                                  (unsigned short*)d_out);
}
```

```cpp
#include <hip/hip_runtime.h>
#include <hip/hip_bf16.h>
#include <stdint.h>

typedef __attribute__((ext_vector_type(4))) unsigned u32x4;
typedef __attribute__((ext_vector_type(4))) float f32x4;
typedef __attribute__((ext_vector_type(2))) float f32x2;
typedef __attribute__((ext_vector_type(2))) double f64x2;
typedef __attribute__((ext_vector_type(2))) unsigned u32x2;
typedef __attribute__((ext_vector_type(2))) __bf16 bf16x2;
typedef __attribute__((address_space(1))) unsigned long long gu64;

#define CFG_NS 24
#define CFG_AHEAD 4
#define CFG_MINCLAIM 2
#define CFG_LEAD 1
constexpr int NS = CFG_NS;
constexpr int AHEAD = CFG_AHEAD;
constexpr int MINCLAIM = CFG_MINCLAIM;

constexpr int BATCH = 4, HKV = 8, SK = 16384, D = 256;
constexpr int NGRP = BATCH * HKV;
constexpr int NWAVE = 8, NSPLIT = 8, NTHR = (NWAVE + 1) * 64;
constexpr int UBYTES = 8 * D * 2;
constexpr int RUN = SK / (8 * NWAVE * NSPLIT);
constexpr int NDYN = (RUN - NS) * NWAVE * NSPLIT;
constexpr int NTICK = NDYN + 128;
constexpr int SLOTB = 8192 + 128;
constexpr int GRPWS = NSPLIT * SLOTB;
constexpr int CTRL = 256;
static_assert(NS >= 3 + CFG_LEAD && NDYN > 0 && NDYN + 64 * NSPLIT < 65535 && AHEAD + NWAVE <= 48 && MINCLAIM <= AHEAD, "unit split");
constexpr float C2 = 0.0625f * 1.4426950408889634f;
constexpr int RSTR = 258;
constexpr unsigned OOB = 0x40000000u, EMPTY = 0xffffffffu;

__device__ __forceinline__ float dot2(unsigned k, unsigned q, float acc) {
  return __builtin_amdgcn_fdot2_f32_bf16(__builtin_bit_cast(bf16x2, k), __builtin_bit_cast(bf16x2, q), acc, false);
}
template <int CTRLW> __device__ __forceinline__ float dpp(float x) {
  return __builtin_bit_cast(float, __builtin_amdgcn_mov_dpp(__builtin_bit_cast(int, x), CTRLW, 0xf, 0xf, true));
}
template <int CTRLW> __device__ __forceinline__ double dpp(double x) {
  const u32x2 b = __builtin_bit_cast(u32x2, x);
  return __builtin_bit_cast(double, u32x2{(unsigned)__builtin_amdgcn_mov_dpp((int)b[0], CTRLW, 0xf, 0xf, true),
                                          (unsigned)__builtin_amdgcn_mov_dpp((int)b[1], CTRLW, 0xf, 0xf, true)});
}
constexpr int XOR1 = 0xB1, XOR2 = 0x4E, XOR7 = 0x141, XOR8 = 0x128, BCAST = 0x150;
__device__ __forceinline__ float xrow_max(float x) {
  auto s = __builtin_amdgcn_permlane16_swap(__float_as_uint(x), __float_as_uint(x), false, false);
  x = fmaxf(__uint_as_float(s[0]), __uint_as_float(s[1]));
  auto t = __builtin_amdgcn_permlane32_swap(__float_as_uint(x), __float_as_uint(x), false, false);
  return fmaxf(__uint_as_float(t[0]), __uint_as_float(t[1]));
}
__device__ __forceinline__ double swap16_sum(double a) {
  const u32x2 x = __builtin_bit_cast(u32x2, a);
  auto lo = __builtin_amdgcn_permlane16_swap(x[0], x[0], false, false);
  auto hi = __builtin_amdgcn_permlane16_swap(x[1], x[1], false, false);
  return __builtin_bit_cast(double, u32x2{lo[0], hi[0]}) + __builtin_bit_cast(double, u32x2{lo[1], hi[1]});
}
__device__ __forceinline__ double swap32_sum(double a, double b) {
  const u32x2 x = __builtin_bit_cast(u32x2, a), y = __builtin_bit_cast(u32x2, b);
  auto lo = __builtin_amdgcn_permlane32_swap(x[0], y[0], false, false);
  auto hi = __builtin_amdgcn_permlane32_swap(x[1], y[1], false, false);
  return __builtin_bit_cast(double, u32x2{lo[0], hi[0]}) + __builtin_bit_cast(double, u32x2{lo[1], hi[1]});
}
__device__ __forceinline__ float rdlane(float x, int l) {
  return __builtin_bit_cast(float, __builtin_amdgcn_readlane(__builtin_bit_cast(int, x), l));
}
__device__ __forceinline__ double rdlane(double x, int l) {
  const u32x2 b = __builtin_bit_cast(u32x2, x);
  return __builtin_bit_cast(double, u32x2{(unsigned)__builtin_amdgcn_readlane((int)b[0], l), (unsigned)__builtin_amdgcn_readlane((int)b[1], l)});
}
__device__ __forceinline__ int expo(float d) { return (int)fmaxf(d, -4000.f); }
__device__ __forceinline__ double pow2d(float d) { return __builtin_amdgcn_ldexp(1.0, expo(d)); }
__device__ __forceinline__ float pow2f(float d) { return __builtin_amdgcn_ldexpf(1.f, expo(d)); }
__device__ __forceinline__ u32x4 granules(double x, unsigned tag) {
  const u32x2 b = __builtin_bit_cast(u32x2, x);
  return u32x4{b[0], tag, b[1], tag};
}
__device__ __forceinline__ void lds_barrier() {
  asm volatile("s_waitcnt lgkmcnt(0)" ::: "memory");
  __builtin_amdgcn_s_barrier();
  asm volatile("" ::: "memory");
}
#define PIN() __builtin_amdgcn_sched_barrier(0)
#define RLX_AGENT __ATOMIC_RELAXED, __HIP_MEMORY_SCOPE_AGENT
#define RLX_WG __ATOMIC_RELAXED, __HIP_MEMORY_SCOPE_WORKGROUP

__global__ void __launch_bounds__(NTHR, 3) attn_decode(const u32x4* __restrict__ q, const char* __restrict__ k,
                                                       const char* __restrict__ v, char* __restrict__ ws,
                                                       unsigned short* __restrict__ out) {
  __shared__ __attribute__((aligned(16))) double rec[NWAVE][2][RSTR];
  __shared__ __attribute__((aligned(16))) float mrec[2][NWAVE];
  __shared__ unsigned tick[NTICK];
  __shared__ unsigned head[64];
  __shared__ unsigned go, tag_s;
  const int tid = threadIdx.x, lane = tid & 63, wave = __builtin_amdgcn_readfirstlane(tid >> 6);
  const int grp = blockIdx.x / NSPLIT, split = blockIdx.x % NSPLIT;
  const auto rw = __builtin_amdgcn_make_buffer_rsrc((void*)(ws + NGRP * CTRL + (size_t)grp * GRPWS), 0, GRPWS, 0x00020000);

  if (wave == NWAVE) {
    gu64* const wq = (gu64*)(ws + grp * CTRL);
    gu64* const we = (gu64*)(ws + grp * CTRL + 128);
    unsigned long long e_in = __hip_atomic_load(we, RLX_AGENT), w_in = __hip_atomic_load(wq, RLX_AGENT);
    PIN();
    __builtin_amdgcn_s_setprio(3);
#pragma unroll
    for (int i = 0; i < NTICK / 64; ++i) tick[i * 64 + lane] = EMPTY;
    head[lane] = 0;
    go = 0;
    lds_barrier();
    asm volatile("" : "+v"(e_in), "+v"(w_in));
    unsigned zero = 0;
    asm volatile("" : "+v"(zero));
    gu64* const wql = wq + zero;
    const unsigned long long epoch = (e_in + 1) & 0xffffffffffffull;
    const unsigned tag = (unsigned)epoch;
    tag_s = tag;
    if (lane == 0 && (w_in >> 16) != epoch) {
      unsigned long long expect = w_in;
      __hip_atomic_compare_exchange_strong(wql, &expect, epoch << 16, __ATOMIC_RELAXED, __ATOMIC_RELAXED, __HIP_MEMORY_SCOPE_AGENT);
      asm volatile("s_waitcnt vmcnt(0)" ::: "memory");
    }
    while (__builtin_amdgcn_readfirstlane(__hip_atomic_load(&go, RLX_WG)) == 0) __builtin_amdgcn_s_sleep(8);
    for (int filled = 0;;) {
      const int need = __builtin_amdgcn_readfirstlane(__hip_atomic_load(&head[0], RLX_WG)) + AHEAD - filled;
      if (need < MINCLAIM) { __builtin_amdgcn_s_sleep(2); continue; }
      unsigned long long got = 0;
      if (lane == 0) got = __hip_atomic_fetch_add(wql, (unsigned long long)need, RLX_AGENT);
      asm volatile("" :: "v"(got));
      const int base = __builtin_amdgcn_readfirstlane((unsigned)got) & 0xffff;
      const bool last = base + need >= NDYN;
      if (lane < need || last) __hip_atomic_store(&tick[filled + lane], lane < need ? base + lane : 0xffffu, RLX_WG);
      filled += need;
      if (last) break;
    }
    lds_barrier();

    u32x2 gv[NSPLIT + 1];
    bool ok[NSPLIT + 1];
#pragma unroll
    for (int i = 0; i <= NSPLIT; ++i) { ok[i] = i == NSPLIT && lane >= 16; gv[i] = u32x2{0, 0}; }
    for (int spin = 0; spin < (1 << 18); ++spin) {
      u32x4 nv[NSPLIT + 1];
#pragma unroll
      for (int i = 0; i <= NSPLIT; ++i) {
        const unsigned off = i < NSPLIT ? i * SLOTB + split * 1024 + lane * 16 : (lane >> 1) * SLOTB + 8192 + (split >> 2) * 32 + (lane & 1) * 16;
        nv[i] = __builtin_amdgcn_raw_buffer_load_b128(rw, ok[i] ? OOB : off, 0, 16);
      }
      PIN();
      bool all = true;
#pragma unroll
      for (int i = 0; i <= NSPLIT; ++i) {
        const bool fresh = (nv[i][1] == tag) & (nv[i][3] == tag) & !ok[i];
        gv[i][0] = fresh ? nv[i][0] : gv[i][0];
        gv[i][1] = fresh ? nv[i][2] : gv[i][1];
        ok[i] |= fresh;
        all &= ok[i];
      }
      if (__all(all)) break;
      __builtin_amdgcn_s_sleep(1);
      asm volatile("" ::: "memory");
    }
    float ms[NSPLIT], Mx = -INFINITY;
#pragma unroll
    for (int i = 0; i < NSPLIT; ++i) { ms[i] = rdlane(__uint_as_float(gv[NSPLIT][0]), 2 * i + 1); Mx = fmaxf(Mx, ms[i]); }
    const float Ms = (Mx == -INFINITY) ? 0.f : Mx;
    double a = 0.0, ls = 0.0;
#pragma unroll
    for (int i = 0; i < NSPLIT; ++i) {
      const double wt = pow2d(ms[i] - Ms);
      a = __builtin_fma(__builtin_bit_cast(double, gv[i]), wt, a);
      ls = __builtin_fma(rdlane(__builtin_bit_cast(double, gv[NSPLIT]), 2 * i), wt, ls);
    }
    const __bf16 res = (__bf16)((float)a / (float)ls);
    out[(grp * 2 + (split >> 2)) * D + (split & 3) * 64 + lane] = __builtin_bit_cast(unsigned short, res);
    if (split == 0 && lane == 0) __hip_atomic_store(we, epoch, RLX_AGENT);
    return;
  }

  const int r = lane >> 3, c = lane & 7;
  const char* kg = k + (size_t)grp * SK * D * 2;
  const char* vg = v + (size_t)grp * SK * D * 2;
  const unsigned kvo = r * 512 + c * 16, vvo = (lane >> 5) * 2048 + (lane & 31) * 16;
  u32x4 kb[4], vb[4], qf[2][4];
  int nxt = split * (RUN * NWAVE) + wave;
  {
    const auto rk = __builtin_amdgcn_make_buffer_rsrc((void*)kg, 0, SK * D * 2, 0x00020000);
    const auto rv = __builtin_amdgcn_make_buffer_rsrc((void*)vg, 0, SK * D * 2, 0x00020000);
    const int so = nxt * UBYTES;
#pragma unroll
    for (int g = 0; g < 2; ++g)
#pragma unroll
      for (int i = 0; i < 4; ++i) { qf[g][i] = q[(grp * 2 + g) * 32 + 8 * i + c]; PIN(); }
#pragma unroll
    for (int j = 0; j < 4; ++j) { kb[j] = __builtin_amdgcn_raw_buffer_load_b128(rk, kvo + j * 128, so, 2); PIN(); }
#pragma unroll
    for (int j = 0; j < 4; ++j) { vb[j] = __builtin_amdgcn_raw_buffer_load_b128(rv, vvo + j * 512, so, 2); PIN(); }
  }
  lds_barrier();
  PIN();

  float m = -INFINITY, l = 0.f;
  f32x2 acc[2][4];
#pragma unroll
  for (int g = 0; g < 2; ++g)
#pragma unroll
    for (int i = 0; i < 4; ++i) acc[g][i] = f32x2{0.f, 0.f};
  unsigned tki = 0, tkv = 0;

  auto step = [&](bool own) __attribute__((always_inline)) {
    const int so = (nxt >= 0 ? nxt : 0) * UBYTES, nrec = nxt >= 0 ? SK * D * 2 : 0;
    const auto rk = __builtin_amdgcn_make_buffer_rsrc((void*)kg, 0, nrec, 0x00020000);
    const auto rv = __builtin_amdgcn_make_buffer_rsrc((void*)vg, 0, nrec, 0x00020000);
    float d0 = 0.f, d1 = 0.f;
#pragma unroll
    for (int i = 0; i < 4; ++i) {
      const u32x4 kk = kb[i];
#pragma unroll
      for (int e = 0; e < 4; ++e) { d0 = dot2(kk[e], qf[0][i][e], d0); d1 = dot2(kk[e], qf[1][i][e], d1); }
      PIN();
      kb[i] = __builtin_amdgcn_raw_buffer_load_b128(rk, kvo + i * 128, so, 2);
      PIN();
    }
    const float t1 = d0 + dpp<XOR7>(d0), t2 = d1 + dpp<XOR7>(d1);
    float s = (c & 4) ? t2 : t1;
    s += dpp<XOR2>(s);
    s = (s + dpp<XOR1>(s)) * C2;
    const float bm = __builtin_ceilf(xrow_max(fmaxf(s, dpp<XOR8>(s))));
    const float mn = fmaxf(m, bm);
    float p;
    if (own) {
      const float ref = (bm == -INFINITY) ? 0.f : bm;
      p = __builtin_amdgcn_ldexpf(__builtin_amdgcn_exp2f(s - ref), expo(ref - mn));
      l = p;
    } else {
      const float msub = (mn == -INFINITY) ? 0.f : mn;
      const float alpha = __builtin_amdgcn_exp2f(m - msub);
      p = __builtin_amdgcn_exp2f(s - msub);
      l = fmaf(l, alpha, p);
      const float a0 = rdlane(alpha, 0), a1 = rdlane(alpha, 4);
#pragma unroll
      for (int i = 0; i < 4; ++i) { acc[0][i] *= a0; acc[1][i] *= a1; }
    }
    m = mn;
    const auto pr = __builtin_amdgcn_permlane16_swap(__float_as_uint(p), __float_as_uint(p), false, false);
    const float pa = __uint_as_float(pr[0]), pb = __uint_as_float(pr[1]);
    auto pv = [&](int j, float p0, float p1) __attribute__((always_inline)) {
      const u32x4 vv = vb[j];
#pragma unroll
      for (int e = 0; e < 4; ++e) {
        const f32x2 vf = f32x2{__builtin_bit_cast(float, vv[e] << 16), __builtin_bit_cast(float, vv[e] & 0xffff0000u)};
        if (own && j == 0) { acc[0][e] = vf * p0; acc[1][e] = vf * p1; }
        else {
          acc[0][e] = __builtin_elementwise_fma(vf, f32x2{p0, p0}, acc[0][e]);
          acc[1][e] = __builtin_elementwise_fma(vf, f32x2{p1, p1}, acc[1][e]);
        }
      }
      asm volatile("" : "+v"(acc[0][0]), "+v"(acc[0][1]), "+v"(acc[0][2]), "+v"(acc[0][3]), "+v"(acc[1][0]), "+v"(acc[1][1]), "+v"(acc[1][2]), "+v"(acc[1][3]));
      PIN();
      vb[j] = __builtin_amdgcn_raw_buffer_load_b128(rv, vvo + j * 512, so, 2);
      PIN();
    };
    pv(0, dpp<BCAST + 0>(pa), dpp<BCAST + 4>(pa));
    pv(1, dpp<BCAST + 8>(pa), dpp<BCAST + 12>(pa));
    if (own) { tki = __hip_atomic_fetch_add(&head[lane], 1u, RLX_WG); PIN(); }
    pv(2, dpp<BCAST + 0>(pb), dpp<BCAST + 4>(pb));
    if (own) { tki = __builtin_amdgcn_readfirstlane(tki); tkv = __hip_atomic_load(&tick[tki], RLX_WG); PIN(); }
    pv(3, dpp<BCAST + 8>(pb), dpp<BCAST + 12>(pb));
  };

#pragma unroll 1
  for (int t = 0; t < NS - 2; ++t) {
    if (t == NS - 2 - CFG_LEAD) __hip_atomic_store(&go, 1u, RLX_WG);
    nxt += NWAVE;
    PIN();
    step(false);
  }
  nxt += NWAVE;
  double L = l, A[2][8];
#pragma unroll
  for (int g = 0; g < 2; ++g)
#pragma unroll
    for (int i = 0; i < 8; ++i) A[g][i] = acc[g][i >> 1][i & 1];
#pragma unroll 1
  for (;;) {
    const float mo = m;
    PIN();
    step(true);
    if (__any(m != mo)) {
      const double wa = pow2d(mo - m);
      L *= wa;
#pragma unroll
      for (int g = 0; g < 2; ++g) {
        const double wag = rdlane(wa, 4 * g);
#pragma unroll
        for (int i = 0; i < 8; ++i) A[g][i] *= wag;
      }
    }
    L += l;
#pragma unroll
    for (int g = 0; g < 2; ++g) {
#pragma unroll
      for (int i = 0; i < 8; ++i) A[g][i] += acc[g][i >> 1][i & 1];
      asm volatile("" : "+v"(A[g][0]), "+v"(A[g][1]), "+v"(A[g][2]), "+v"(A[g][3]), "+v"(A[g][4]), "+v"(A[g][5]), "+v"(A[g][6]), "+v"(A[g][7]));
    }
    if (nxt < 0) break;
    unsigned cq = __builtin_amdgcn_readfirstlane(tkv);
    while (cq == EMPTY) {
      __builtin_amdgcn_s_sleep(1);
      cq = __builtin_amdgcn_readfirstlane(__hip_atomic_load(&tick[tki], RLX_WG));
    }
    nxt = cq < NDYN ? ((cq >> 3) & 7) * (RUN * NWAVE) + (NS + (cq >> 6)) * NWAVE + (cq & 7) : -1;
  }

  {
    double tl = L;
    tl += dpp<XOR8>(tl); tl = swap16_sum(tl); tl = swap32_sum(tl, tl);
    if (lane == 0 || lane == 4) { mrec[lane >> 2][wave] = m; rec[wave][lane >> 2][D] = tl; }
    double* dst = &rec[wave][lane >> 5][(lane & 31) * 8];
#pragma unroll
    for (int i = 0; i < 4; ++i)
      *reinterpret_cast<f64x2*>(dst + 2 * i) = f64x2{swap32_sum(A[0][2 * i], A[1][2 * i]), swap32_sum(A[0][2 * i + 1], A[1][2 * i + 1])};
  }
  asm volatile("" :: "v"(kb[0]), "v"(kb[1]), "v"(kb[2]), "v"(kb[3]), "v"(vb[0]), "v"(vb[1]), "v"(vb[2]), "v"(vb[3]));
  lds_barrier();
  {
    const unsigned tag = tag_s;
    const int g = wave >> 2;
    const f32x4 ma = *reinterpret_cast<const f32x4*>(&mrec[g][0]), mb = *reinterpret_cast<const f32x4*>(&mrec[g][4]);
    double ov[NWAVE], lv[NWAVE];
#pragma unroll
    for (int w = 0; w < NWAVE; ++w) { ov[w] = rec[w][g][tid & 255]; lv[w] = rec[w][g][D]; }
    const float Mw = fmaxf(fmaxf(fmaxf(ma[0], ma[1]), fmaxf(ma[2], ma[3])), fmaxf(fmaxf(mb[0], mb[1]), fmaxf(mb[2], mb[3])));
    const float Ms = (Mw == -INFINITY) ? 0.f : Mw;
    double a = 0.0, ls = 0.0;
#pragma unroll
    for (int w = 0; w < NWAVE; ++w) {
      const double wt = pow2d((w < 4 ? ma[w & 3] : mb[w & 3]) - Ms);
      a = __builtin_fma(ov[w], wt, a); ls = __builtin_fma(lv[w], wt, ls);
    }
    __builtin_amdgcn_raw_buffer_store_b128(granules(a, tag), rw, split * SLOTB + tid * 16, 0, 16);
    const u32x4 st = lane ? u32x4{__float_as_uint(Mw), tag, 0u, tag} : granules(ls, tag);
    __builtin_amdgcn_raw_buffer_store_b128(st, rw, ((wave & 3) == 0 && lane < 2) ? split * SLOTB + 8192 + g * 32 + lane * 16 : OOB, 0, 16);
  }
}

extern "C" void kernel_launch(void* const* d_in, const int* in_sizes, int n_in, void* d_out, int out_size, void* d_ws, size_t ws_size,
                              hipStream_t stream) {
  attn_decode<<<NGRP * NSPLIT, NTHR, 0, stream>>>((const u32x4*)d_in[0], (const char*)d_in[1], (const char*)d_in[2], (char*)d_ws,
                                                  (unsigned short*)d_out);
}
```

```cpp
#include <hip/hip_runtime.h>
#include <hip/hip_bf16.h>
#include <stdint.h>

typedef __attribute__((ext_vector_type(4))) unsigned u32x4;
typedef __attribute__((ext_vector_type(4))) float f32x4;
typedef __attribute__((ext_vector_type(2))) float f32x2;
typedef __attribute__((ext_vector_type(2))) double f64x2;
typedef __attribute__((ext_vector_type(2))) unsigned u32x2;
typedef __attribute__((ext_vector_type(2))) __bf16 bf16x2;
typedef __attribute__((address_space(1))) unsigned long long gu64;

#define CFG_NS 20
#define CFG_AHEAD 4
#define CFG_MINCLAIM 2
#define CFG_LEAD 1
#define CFG_KAUX 2
#define CFG_VAUX 0
constexpr int NS = CFG_NS;
constexpr int AHEAD = CFG_AHEAD;
constexpr int MINCLAIM = CFG_MINCLAIM;

constexpr int BATCH = 4, HKV = 8, SK = 16384, D = 256;
constexpr int NGRP = BATCH * HKV;
constexpr int NWAVE = 8, NSPLIT = 8, NTHR = (NWAVE + 1) * 64;
constexpr int UBYTES = 8 * D * 2;
constexpr int RUN = SK / (8 * NWAVE * NSPLIT);
constexpr int NDYN = (RUN - NS) * NWAVE * NSPLIT;
constexpr int NTICK = NDYN + 128;
constexpr int SLOTB = 8192 + 128;
constexpr int GRPWS = NSPLIT * SLOTB;
constexpr int CTRL = 256;
static_assert(NS >= 3 + CFG_LEAD && NDYN > 0 && NDYN + 64 * NSPLIT < 65535 && AHEAD + NWAVE <= 48 && MINCLAIM <= AHEAD, "unit split");
constexpr float C2 = 0.0625f * 1.4426950408889634f;
constexpr int RSTR = 258;
constexpr unsigned OOB = 0x40000000u, EMPTY = 0xffffffffu;

__device__ __forceinline__ float dot2(unsigned k, unsigned q, float acc) {
  return __builtin_amdgcn_fdot2_f32_bf16(__builtin_bit_cast(bf16x2, k), __builtin_bit_cast(bf16x2, q), acc, false);
}
template <int CTRLW> __device__ __forceinline__ float dpp(float x) {
  return __builtin_bit_cast(float, __builtin_amdgcn_mov_dpp(__builtin_bit_cast(int, x), CTRLW, 0xf, 0xf, true));
}
template <int CTRLW> __device__ __forceinline__ double dpp(double x) {
  const u32x2 b = __builtin_bit_cast(u32x2, x);
  return __builtin_bit_cast(double, u32x2{(unsigned)__builtin_amdgcn_mov_dpp((int)b[0], CTRLW, 0xf, 0xf, true),
                                          (unsigned)__builtin_amdgcn_mov_dpp((int)b[1], CTRLW, 0xf, 0xf, true)});
}
constexpr int XOR1 = 0xB1, XOR2 = 0x4E, XOR7 = 0x141, XOR8 = 0x128, BCAST = 0x150;
__device__ __forceinline__ float xrow_max(float x) {
  auto s = __builtin_amdgcn_permlane16_swap(__float_as_uint(x), __float_as_uint(x), false, false);
  x = fmaxf(__uint_as_float(s[0]), __uint_as_float(s[1]));
  auto t = __builtin_amdgcn_permlane32_swap(__float_as_uint(x), __float_as_uint(x), false, false);
  return fmaxf(__uint_as_float(t[0]), __uint_as_float(t[1]));
}
__device__ __forceinline__ double swap16_sum(double a) {
  const u32x2 x = __builtin_bit_cast(u32x2, a);
  auto lo = __builtin_amdgcn_permlane16_swap(x[0], x[0], false, false);
  auto hi = __builtin_amdgcn_permlane16_swap(x[1], x[1], false, false);
  return __builtin_bit_cast(double, u32x2{lo[0], hi[0]}) + __builtin_bit_cast(double, u32x2{lo[1], hi[1]});
}
__device__ __forceinline__ double swap32_sum(double a, double b) {
  const u32x2 x = __builtin_bit_cast(u32x2, a), y = __builtin_bit_cast(u32x2, b);
  auto lo = __builtin_amdgcn_permlane32_swap(x[0], y[0], false, false);
  auto hi = __builtin_amdgcn_permlane32_swap(x[1], y[1], false, false);
  return __builtin_bit_cast(double, u32x2{lo[0], hi[0]}) + __builtin_bit_cast(double, u32x2{lo[1], hi[1]});
}
__device__ __forceinline__ float rdlane(float x, int l) {
  return __builtin_bit_cast(float, __builtin_amdgcn_readlane(__builtin_bit_cast(int, x), l));
}
__device__ __forceinline__ double rdlane(double x, int l) {
  const u32x2 b = __builtin_bit_cast(u32x2, x);
  return __builtin_bit_cast(double, u32x2{(unsigned)__builtin_amdgcn_readlane((int)b[0], l), (unsigned)__builtin_amdgcn_readlane((int)b[1], l)});
}
__device__ __forceinline__ int expo(float d) { return (int)fmaxf(d, -4000.f); }
__device__ __forceinline__ double pow2d(float d) { return __builtin_amdgcn_ldexp(1.0, expo(d)); }
__device__ __forceinline__ float pow2f(float d) { return __builtin_amdgcn_ldexpf(1.f, expo(d)); }
__device__ __forceinline__ u32x4 granules(double x, unsigned tag) {
  const u32x2 b = __builtin_bit_cast(u32x2, x);
  return u32x4{b[0], tag, b[1], tag};
}
__device__ __forceinline__ void lds_barrier() {
  asm volatile("s_waitcnt lgkmcnt(0)" ::: "memory");
  __builtin_amdgcn_s_barrier();
  asm volatile("" ::: "memory");
}
#define PIN() __builtin_amdgcn_sched_barrier(0)
#define RLX_AGENT __ATOMIC_RELAXED, __HIP_MEMORY_SCOPE_AGENT
#define RLX_WG __ATOMIC_RELAXED, __HIP_MEMORY_SCOPE_WORKGROUP

__global__ void __launch_bounds__(NTHR, 3) attn_decode(const u32x4* __restrict__ q, const char* __restrict__ k,
                                                       const char* __restrict__ v, char* __restrict__ ws,
                                                       unsigned short* __restrict__ out) {
  __shared__ __attribute__((aligned(16))) double rec[NWAVE][2][RSTR];
  __shared__ __attribute__((aligned(16))) float mrec[2][NWAVE];
  __shared__ unsigned tick[NTICK];
  __shared__ unsigned head[64];
  __shared__ unsigned go, tag_s;
  const int tid = threadIdx.x, lane = tid & 63, wave = __builtin_amdgcn_readfirstlane(tid >> 6);
  const int grp = blockIdx.x / NSPLIT, split = blockIdx.x % NSPLIT;
  const auto rw = __builtin_amdgcn_make_buffer_rsrc((void*)(ws + NGRP * CTRL + (size_t)grp * GRPWS), 0, GRPWS, 0x00020000);

  if (wave == NWAVE) {
    gu64* const wq = (gu64*)(ws + grp * CTRL);
    gu64* const we = (gu64*)(ws + grp * CTRL + 128);
    unsigned long long e_in = __hip_atomic_load(we, RLX_AGENT), w_in = __hip_atomic_load(wq, RLX_AGENT);
    PIN();
    __builtin_amdgcn_s_setprio(3);
#pragma unroll
    for (int i = 0; i < NTICK / 64; ++i) tick[i * 64 + lane] = EMPTY;
    head[lane] = 0;
    go = 0;
    lds_barrier();
    asm volatile("" : "+v"(e_in), "+v"(w_in));
    unsigned zero = 0;
    asm volatile("" : "+v"(zero));
    gu64* const wql = wq + zero;
    const unsigned long long epoch = (e_in + 1) & 0xffffffffffffull;
    const unsigned tag = (unsigned)epoch;
    tag_s = tag;
    if (lane == 0 && (w_in >> 16) != epoch) {
      unsigned long long expect = w_in;
      __hip_atomic_compare_exchange_strong(wql, &expect, epoch << 16, __ATOMIC_RELAXED, __ATOMIC_RELAXED, __HIP_MEMORY_SCOPE_AGENT);
      asm volatile("s_waitcnt vmcnt(0)" ::: "memory");
    }
    while (__builtin_amdgcn_readfirstlane(__hip_atomic_load(&go, RLX_WG)) == 0) __builtin_amdgcn_s_sleep(8);
    for (int filled = 0;;) {
      const int need = __builtin_amdgcn_readfirstlane(__hip_atomic_load(&head[0], RLX_WG)) + AHEAD - filled;
      if (need < MINCLAIM) { __builtin_amdgcn_s_sleep(2); continue; }
      unsigned long long got = 0;
      if (lane == 0) got = __hip_atomic_fetch_add(wql, (unsigned long long)need, RLX_AGENT);
      asm volatile("" :: "v"(got));
      const int base = __builtin_amdgcn_readfirstlane((unsigned)got) & 0xffff;
      const bool last = base + need >= NDYN;
      if (lane < need || last) __hip_atomic_store(&tick[filled + lane], lane < need ? base + lane : 0xffffu, RLX_WG);
      filled += need;
      if (last) break;
    }
    lds_barrier();

    u32x2 gv[NSPLIT + 1];
    bool ok[NSPLIT + 1];
#pragma unroll
    for (int i = 0; i <= NSPLIT; ++i) { ok[i] = i == NSPLIT && lane >= 16; gv[i] = u32x2{0, 0}; }
    for (int spin = 0; spin < (1 << 18); ++spin) {
      u32x4 nv[NSPLIT + 1];
#pragma unroll
      for (int i = 0; i <= NSPLIT; ++i) {
        const unsigned off = i < NSPLIT ? i * SLOTB + split * 1024 + lane * 16 : (lane >> 1) * SLOTB + 8192 + (split >> 2) * 32 + (lane & 1) * 16;
        nv[i] = __builtin_amdgcn_raw_buffer_load_b128(rw, ok[i] ? OOB : off, 0, 16);
      }
      PIN();
      bool all = true;
#pragma unroll
      for (int i = 0; i <= NSPLIT; ++i) {
        const bool fresh = (nv[i][1] == tag) & (nv[i][3] == tag) & !ok[i];
        gv[i][0] = fresh ? nv[i][0] : gv[i][0];
        gv[i][1] = fresh ? nv[i][2] : gv[i][1];
        ok[i] |= fresh;
        all &= ok[i];
      }
      if (__all(all)) break;
      __builtin_amdgcn_s_sleep(1);
      asm volatile("" ::: "memory");
    }
    float ms[NSPLIT], Mx = -INFINITY;
#pragma unroll
    for (int i = 0; i < NSPLIT; ++i) { ms[i] = rdlane(__uint_as_float(gv[NSPLIT][0]), 2 * i + 1); Mx = fmaxf(Mx, ms[i]); }
    const float Ms = (Mx == -INFINITY) ? 0.f : Mx;
    double a = 0.0, ls = 0.0;
#pragma unroll
    for (int i = 0; i < NSPLIT; ++i) {
      const double wt = pow2d(ms[i] - Ms);
      a = __builtin_fma(__builtin_bit_cast(double, gv[i]), wt, a);
      ls = __builtin_fma(rdlane(__builtin_bit_cast(double, gv[NSPLIT]), 2 * i), wt, ls);
    }
    const __bf16 res = (__bf16)((float)a / (float)ls);
    out[(grp * 2 + (split >> 2)) * D + (split & 3) * 64 + lane] = __builtin_bit_cast(unsigned short, res);
    if (split == 0 && lane == 0) __hip_atomic_store(we, epoch, RLX_AGENT);
    return;
  }

  const int r = lane >> 3, c = lane & 7;
  const char* kg = k + (size_t)grp * SK * D * 2;
  const char* vg = v + (size_t)grp * SK * D * 2;
  const unsigned kvo = r * 512 + c * 16, vvo = (lane >> 5) * 2048 + (lane & 31) * 16;
  u32x4 kb[4], vb[4], qf[2][4];
  int nxt = split * (RUN * NWAVE) + wave;
  {
    const auto rk = __builtin_amdgcn_make_buffer_rsrc((void*)kg, 0, SK * D * 2, 0x00020000);
    const auto rv = __builtin_amdgcn_make_buffer_rsrc((void*)vg, 0, SK * D * 2, 0x00020000);
    const int so = nxt * UBYTES;
#pragma unroll
    for (int g = 0; g < 2; ++g)
#pragma unroll
      for (int i = 0; i < 4; ++i) { qf[g][i] = q[(grp * 2 + g) * 32 + 8 * i + c]; PIN(); }
#pragma unroll
    for (int j = 0; j < 4; ++j) { kb[j] = __builtin_amdgcn_raw_buffer_load_b128(rk, kvo + j * 128, so, CFG_KAUX); PIN(); }
#pragma unroll
    for (int j = 0; j < 4; ++j) { vb[j] = __builtin_amdgcn_raw_buffer_load_b128(rv, vvo + j * 512, so, CFG_VAUX); PIN(); }
  }
  lds_barrier();
  PIN();

  float m = -INFINITY, l = 0.f;
  f32x2 acc[2][4];
#pragma unroll
  for (int g = 0; g < 2; ++g)
#pragma unroll
    for (int i = 0; i < 4; ++i) acc[g][i] = f32x2{0.f, 0.f};
  unsigned tki = 0, tkv = 0;

  auto step = [&](bool own) __attribute__((always_inline)) {
    const int so = (nxt >= 0 ? nxt : 0) * UBYTES, nrec = nxt >= 0 ? SK * D * 2 : 0;
    const auto rk = __builtin_amdgcn_make_buffer_rsrc((void*)kg, 0, nrec, 0x00020000);
    const auto rv = __builtin_amdgcn_make_buffer_rsrc((void*)vg, 0, nrec, 0x00020000);
    float d0 = 0.f, d1 = 0.f;
#pragma unroll
    for (int i = 0; i < 4; ++i) {
      const u32x4 kk = kb[i];
#pragma unroll
      for (int e = 0; e < 4; ++e) { d0 = dot2(kk[e], qf[0][i][e], d0); d1 = dot2(kk[e], qf[1][i][e], d1); }
      PIN();
      kb[i] = __builtin_amdgcn_raw_buffer_load_b128(rk, kvo + i * 128, so, CFG_KAUX);
      PIN();
    }
    const float t1 = d0 + dpp<XOR7>(d0), t2 = d1 + dpp<XOR7>(d1);
    float s = (c & 4) ? t2 : t1;
    s += dpp<XOR2>(s);
    s = (s + dpp<XOR1>(s)) * C2;
    const float bm = __builtin_ceilf(xrow_max(fmaxf(s, dpp<XOR8>(s))));
    const float mn = fmaxf(m, bm);
    float p;
    if (own) {
      const float ref = (bm == -INFINITY) ? 0.f : bm;
      p = __builtin_amdgcn_ldexpf(__builtin_amdgcn_exp2f(s - ref), expo(ref - mn));
      l = p;
    } else {
      const float msub = (mn == -INFINITY) ? 0.f : mn;
      const float alpha = __builtin_amdgcn_exp2f(m - msub);
      p = __builtin_amdgcn_exp2f(s - msub);
      l = fmaf(l, alpha, p);
      const float a0 = rdlane(alpha, 0), a1 = rdlane(alpha, 4);
#pragma unroll
      for (int i = 0; i < 4; ++i) { acc[0][i] *= a0; acc[1][i] *= a1; }
    }
    m = mn;
    const auto pr = __builtin_amdgcn_permlane16_swap(__float_as_uint(p), __float_as_uint(p), false, false);
    const float pa = __uint_as_float(pr[0]), pb = __uint_as_float(pr[1]);
    auto pv = [&](int j, float p0, float p1) __attribute__((always_inline)) {
      const u32x4 vv = vb[j];
#pragma unroll
      for (int e = 0; e < 4; ++e) {
        const f32x2 vf = f32x2{__builtin_bit_cast(float, vv[e] << 16), __builtin_bit_cast(float, vv[e] & 0xffff0000u)};
        if (own && j == 0) { acc[0][e] = vf * p0; acc[1][e] = vf * p1; }
        else {
          acc[0][e] = __builtin_elementwise_fma(vf, f32x2{p0, p0}, acc[0][e]);
          acc[1][e] = __builtin_elementwise_fma(vf, f32x2{p1, p1}, acc[1][e]);
        }
      }
      asm volatile("" : "+v"(acc[0][0]), "+v"(acc[0][1]), "+v"(acc[0][2]), "+v"(acc[0][3]), "+v"(acc[1][0]), "+v"(acc[1][1]), "+v"(acc[1][2]), "+v"(acc[1][3]));
      PIN();
      vb[j] = __builtin_amdgcn_raw_buffer_load_b128(rv, vvo + j * 512, so, CFG_VAUX);
      PIN();
    };
    pv(0, dpp<BCAST + 0>(pa), dpp<BCAST + 4>(pa));
    pv(1, dpp<BCAST + 8>(pa), dpp<BCAST + 12>(pa));
    if (own) { tki = __hip_atomic_fetch_add(&head[lane], 1u, RLX_WG); PIN(); }
    pv(2, dpp<BCAST + 0>(pb), dpp<BCAST + 4>(pb));
    if (own) { tki = __builtin_amdgcn_readfirstlane(tki); tkv = __hip_atomic_load(&tick[tki], RLX_WG); PIN(); }
    pv(3, dpp<BCAST + 8>(pb), dpp<BCAST + 12>(pb));
  };

#pragma unroll 1
  for (int t = 0; t < NS - 2; ++t) {
    if (t == NS - 2 - CFG_LEAD) __hip_atomic_store(&go, 1u, RLX_WG);
    nxt += NWAVE;
    PIN();
    step(false);
  }
  nxt += NWAVE;
  double L = l, A[2][8];
#pragma unroll
  for (int g = 0; g < 2; ++g)
#pragma unroll
    for (int i = 0; i < 8; ++i) A[g][i] = acc[g][i >> 1][i & 1];
#pragma unroll 1
  for (;;) {
    const float mo = m;
    PIN();
    step(true);
    if (__any(m != mo)) {
      const double wa = pow2d(mo - m);
      L *= wa;
#pragma unroll
      for (int g = 0; g < 2; ++g) {
        const double wag = rdlane(wa, 4 * g);
#pragma unroll
        for (int i = 0; i < 8; ++i) A[g][i] *= wag;
      }
    }
    L += l;
#pragma unroll
    for (int g = 0; g < 2; ++g) {
#pragma unroll
      for (int i = 0; i < 8; ++i) A[g][i] += acc[g][i >> 1][i & 1];
      asm volatile("" : "+v"(A[g][0]), "+v"(A[g][1]), "+v"(A[g][2]), "+v"(A[g][3]), "+v"(A[g][4]), "+v"(A[g][5]), "+v"(A[g][6]), "+v"(A[g][7]));
    }
    if (nxt < 0) break;
    unsigned cq = __builtin_amdgcn_readfirstlane(tkv);
    while (cq == EMPTY) {
      __builtin_amdgcn_s_sleep(1);
      cq = __builtin_amdgcn_readfirstlane(__hip_atomic_load(&tick[tki], RLX_WG));
    }
    nxt = cq < NDYN ? ((cq >> 3) & 7) * (RUN * NWAVE) + (NS + (cq >> 6)) * NWAVE + (cq & 7) : -1;
  }

  {
    double tl = L;
    tl += dpp<XOR8>(tl); tl = swap16_sum(tl); tl = swap32_sum(tl, tl);
    if (lane == 0 || lane == 4) { mrec[lane >> 2][wave] = m; rec[wave][lane >> 2][D] = tl; }
    double* dst = &rec[wave][lane >> 5][(lane & 31) * 8];
#pragma unroll
    for (int i = 0; i < 4; ++i)
      *reinterpret_cast<f64x2*>(dst + 2 * i) = f64x2{swap32_sum(A[0][2 * i], A[1][2 * i]), swap32_sum(A[0][2 * i + 1], A[1][2 * i + 1])};
  }
  asm volatile("" :: "v"(kb[0]), "v"(kb[1]), "v"(kb[2]), "v"(kb[3]), "v"(vb[0]), "v"(vb[1]), "v"(vb[2]), "v"(vb[3]));
  lds_barrier();
  {
    const unsigned tag = tag_s;
    const int g = wave >> 2;
    const f32x4 ma = *reinterpret_cast<const f32x4*>(&mrec[g][0]), mb = *reinterpret_cast<const f32x4*>(&mrec[g][4]);
    double ov[NWAVE], lv[NWAVE];
#pragma unroll
    for (int w = 0; w < NWAVE; ++w) { ov[w] = rec[w][g][tid & 255]; lv[w] = rec[w][g][D]; }
    const float Mw = fmaxf(fmaxf(fmaxf(ma[0], ma[1]), fmaxf(ma[2], ma[3])), fmaxf(fmaxf(mb[0], mb[1]), fmaxf(mb[2], mb[3])));
    const float Ms = (Mw == -INFINITY) ? 0.f : Mw;
    double a = 0.0, ls = 0.0;
#pragma unroll
    for (int w = 0; w < NWAVE; ++w) {
      const double wt = pow2d((w < 4 ? ma[w & 3] : mb[w & 3]) - Ms);
      a = __builtin_fma(ov[w], wt, a); ls = __builtin_fma(lv[w], wt, ls);
    }
    __builtin_amdgcn_raw_buffer_store_b128(granules(a, tag), rw, split * SLOTB + tid * 16, 0, 16);
    const u32x4 st = lane ? u32x4{__float_as_uint(Mw), tag, 0u, tag} : granules(ls, tag);
    __builtin_amdgcn_raw_buffer_store_b128(st, rw, ((wave & 3) == 0 && lane < 2) ? split * SLOTB + 8192 + g * 32 + lane * 16 : OOB, 0, 16);
  }
}

extern "C" void kernel_launch(void* const* d_in, const int* in_sizes, int n_in, void* d_out, int out_size, void* d_ws, size_t ws_size,
                              hipStream_t stream) {
  attn_decode<<<NGRP * NSPLIT, NTHR, 0, stream>>>((const u32x4*)d_in[0], (const char*)d_in[1], (const char*)d_in[2], (char*)d_ws,
                                                  (unsigned short*)d_out);
}
```

```cpp
#include <hip/hip_runtime.h>
#include <hip/hip_bf16.h>
#include <stdint.h>

typedef __attribute__((ext_vector_type(4))) unsigned u32x4;
typedef __attribute__((ext_vector_type(4))) float f32x4;
typedef __attribute__((ext_vector_type(2))) float f32x2;
typedef __attribute__((ext_vector_type(2))) double f64x2;
typedef __attribute__((ext_vector_type(2))) unsigned u32x2;
typedef __attribute__((ext_vector_type(2))) __bf16 bf16x2;
typedef __attribute__((address_space(1))) unsigned long long gu64;

constexpr int AMIN = 4, AMAX = 48, LOW = 6;

constexpr int BATCH = 4, HKV = 8, SK = 16384, D = 256;
constexpr int NGRP = BATCH * HKV;
constexpr int NWAVE = 4, NSPLIT = 8, NTHR = (NWAVE + 1) * 64;
constexpr int UBYTES = 8 * D * 2;
constexpr int REG = SK / (8 * NSPLIT), NCH = REG / 2;
constexpr int NTICK = NSPLIT * REG;
constexpr int NPOLL = 4;
constexpr int MLOFF = NSPLIT * NSPLIT * 512;
constexpr int FLOFF = MLOFF + 256;
constexpr int QOFF = FLOFF + 128, EOFF = QOFF + 128;
constexpr int GRPWS = EOFF + 128;
static_assert(AMAX + 3 * NWAVE <= 64 && AMIN >= 1, "tickets");
constexpr float C2 = 0.0625f * 1.4426950408889634f;
constexpr unsigned OOB = 0x40000000u;

__device__ __forceinline__ float dot2(unsigned k, unsigned q, float acc) {
  return __builtin_amdgcn_fdot2_f32_bf16(__builtin_bit_cast(bf16x2, k), __builtin_bit_cast(bf16x2, q), acc, false);
}
template <int CTRLW> __device__ __forceinline__ float dpp(float x) {
  return __builtin_bit_cast(float, __builtin_amdgcn_mov_dpp(__builtin_bit_cast(int, x), CTRLW, 0xf, 0xf, true));
}
template <int CTRLW> __device__ __forceinline__ double dpp(double x) {
  const u32x2 b = __builtin_bit_cast(u32x2, x);
  return __builtin_bit_cast(double, u32x2{(unsigned)__builtin_amdgcn_mov_dpp((int)b[0], CTRLW, 0xf, 0xf, true),
                                          (unsigned)__builtin_amdgcn_mov_dpp((int)b[1], CTRLW, 0xf, 0xf, true)});
}
constexpr int XOR1 = 0xB1, XOR2 = 0x4E, XOR7 = 0x141, XOR8 = 0x128, BCAST = 0x150;
__device__ __forceinline__ float key_max(float x) {
  x = fmaxf(x, dpp<XOR8>(x));
  auto s = __builtin_amdgcn_permlane16_swap(__float_as_uint(x), __float_as_uint(x), false, false);
  x = fmaxf(__uint_as_float(s[0]), __uint_as_float(s[1]));
  auto t = __builtin_amdgcn_permlane32_swap(__float_as_uint(x), __float_as_uint(x), false, false);
  return fmaxf(__uint_as_float(t[0]), __uint_as_float(t[1]));
}
__device__ __forceinline__ float key_sum(float x) {
  x += dpp<XOR8>(x);
  auto s = __builtin_amdgcn_permlane16_swap(__float_as_uint(x), __float_as_uint(x), false, false);
  x = __uint_as_float(s[0]) + __uint_as_float(s[1]);
  auto t = __builtin_amdgcn_permlane32_swap(__float_as_uint(x), __float_as_uint(x), false, false);
  return __uint_as_float(t[0]) + __uint_as_float(t[1]);
}
__device__ __forceinline__ double swap32_sum(double a, double b) {
  const u32x2 x = __builtin_bit_cast(u32x2, a), y = __builtin_bit_cast(u32x2, b);
  auto lo = __builtin_amdgcn_permlane32_swap(x[0], y[0], false, false);
  auto hi = __builtin_amdgcn_permlane32_swap(x[1], y[1], false, false);
  return __builtin_bit_cast(double, u32x2{lo[0], hi[0]}) + __builtin_bit_cast(double, u32x2{lo[1], hi[1]});
}
__device__ __forceinline__ f32x2 swap32_sum(f32x2 a, f32x2 b) {
  const u32x2 x = __builtin_bit_cast(u32x2, a), y = __builtin_bit_cast(u32x2, b);
  auto lo = __builtin_amdgcn_permlane32_swap(x[0], y[0], false, false);
  auto hi = __builtin_amdgcn_permlane32_swap(x[1], y[1], false, false);
  return __builtin_bit_cast(f32x2, u32x2{lo[0], hi[0]}) + __builtin_bit_cast(f32x2, u32x2{lo[1], hi[1]});
}
__device__ __forceinline__ double rdlane(double x, int l) {
  const u32x2 b = __builtin_bit_cast(u32x2, x);
  return __builtin_bit_cast(double, u32x2{(unsigned)__builtin_amdgcn_readlane((int)b[0], l), (unsigned)__builtin_amdgcn_readlane((int)b[1], l)});
}
__device__ __forceinline__ int expo(float d) { return (int)fmaxf(d, -4000.f); }
__device__ __forceinline__ double pow2d(float d) { return __builtin_amdgcn_ldexp(1.0, expo(d)); }
__device__ __forceinline__ u32x4 pack2(double x, double y, unsigned mx, unsigned my) {
  const u32x2 a = __builtin_bit_cast(u32x2, x), b = __builtin_bit_cast(u32x2, y);
  return u32x4{(a[0] & ~1u) | mx, a[1], (b[0] & ~1u) | my, b[1]};
}
__device__ __forceinline__ double mkd(unsigned lo, unsigned hi) { return __builtin_bit_cast(double, u32x2{lo, hi}); }
__device__ __forceinline__ void lds_barrier() {
  asm volatile("s_waitcnt lgkmcnt(0)" ::: "memory");
  __builtin_amdgcn_s_barrier();
  asm volatile("" ::: "memory");
}
template <int N> struct IC { constexpr int operator()() const { return N; } };
#define A10(x) "a" #x "0", "a" #x "1", "a" #x "2", "a" #x "3", "a" #x "4", "a" #x "5", "a" #x "6", "a" #x "7", "a" #x "8", "a" #x "9"
#define AGPRS A10(), A10(1), A10(2), A10(3), A10(4), A10(5), A10(6), A10(7), A10(8), A10(9)
#define PIN() __builtin_amdgcn_sched_barrier(0)
#define RLX_AGENT __ATOMIC_RELAXED, __HIP_MEMORY_SCOPE_AGENT
#define RLX_WG __ATOMIC_RELAXED, __HIP_MEMORY_SCOPE_WORKGROUP

__global__ void __launch_bounds__(NTHR, 1) attn_decode(const u32x4* __restrict__ q, const char* __restrict__ k,
                                                       const char* __restrict__ v, char* __restrict__ ws,
                                                       unsigned short* __restrict__ out) {
  __shared__ __attribute__((aligned(16))) float pw[REG][20];
  __shared__ __attribute__((aligned(16))) f64x2 rec[NWAVE][4][64];
  __shared__ __attribute__((aligned(16))) double lrec[2][NWAVE];
  __shared__ __attribute__((aligned(16))) float mrec[2][NWAVE];
  __shared__ unsigned tick[NTICK];
  __shared__ unsigned kcnt[64];
  __shared__ unsigned head[64];
  __shared__ unsigned fin[64];
  __shared__ unsigned mark[64];
  __shared__ unsigned go, avail;
  asm volatile("" :: "s"(q), "s"(k), "s"(v), "s"(ws), "s"(out));
  const int tid = threadIdx.x, lane = tid & 63, wave = __builtin_amdgcn_readfirstlane(tid >> 6);
  const int grp = blockIdx.x / NSPLIT, split = blockIdx.x % NSPLIT;
  char* const gws = ws + (size_t)grp * GRPWS;
  const auto rw = __builtin_amdgcn_make_buffer_rsrc((void*)gws, 0, GRPWS, 0x00020000);
  const char* kg = k + (size_t)grp * SK * D * 2;
  const char* vg = v + (size_t)grp * SK * D * 2;

  const unsigned pub = ((lane >> 5) * 4 * NSPLIT + split) * 512 + (lane & 31) * 16;
  const unsigned pubml = (lane & 31) == 0 ? MLOFF + (lane >> 5) * 128 + split * 16 : OOB;

  if (wave == NWAVE) {
    const unsigned long long t0 = __builtin_amdgcn_s_memrealtime();
    const unsigned rdo = split * NSPLIT * 512 + lane * 16, rml = lane < NSPLIT ? MLOFF + (split >> 2) * 128 + lane * 16 : OOB;
    __builtin_amdgcn_s_setprio(3);
    kcnt[lane] = 2 * NWAVE;
    head[lane] = 0;
    fin[lane] = 0;
    go = 0;
    avail = 0;
    lds_barrier();
    const u32x2 e_in = __builtin_amdgcn_raw_buffer_load_b64(rw, EOFF, 0, 16);
    u32x4 ow[5], orr[5];
#pragma unroll
    for (int i = 0; i < 4; ++i) {
      ow[i] = __builtin_amdgcn_raw_buffer_load_b128(rw, pub + i * NSPLIT * 512, 0, 16);
      orr[i] = __builtin_amdgcn_raw_buffer_load_b128(rw, rdo + i * 1024, 0, 16);
    }
    ow[4] = __builtin_amdgcn_raw_buffer_load_b128(rw, pubml, 0, 16);
    orr[4] = __builtin_amdgcn_raw_buffer_load_b128(rw, rml, 0, 16);
    PIN();
    const unsigned long long epoch = (__builtin_bit_cast(unsigned long long, e_in) + 1) & 0xffffffffffffull;
    __builtin_amdgcn_raw_buffer_store_b64(__builtin_bit_cast(u32x2, epoch << 16), rw, split == 0 && lane == 0 ? QOFF : OOB, 0, 16);
    unsigned mk = 0, want[10];
#pragma unroll
    for (int i = 0; i < 10; ++i) {
      mk |= (~ow[i >> 1][(i & 1) * 2] & 1u) << i;
      want[i] = (i < 8 || lane < NSPLIT) ? ~orr[i >> 1][(i & 1) * 2] & 1u : 0u;
    }
    mark[lane] = mk;
    asm volatile("s_waitcnt vmcnt(0)" :: "v"(want[0]), "v"(want[1]), "v"(want[2]), "v"(want[3]), "v"(want[4]), "v"(want[5]),
                 "v"(want[6]), "v"(want[7]), "v"(want[8]), "v"(want[9]) : "memory");
    while (__builtin_amdgcn_s_memrealtime() - t0 < 3000 - 100) __builtin_amdgcn_s_sleep(32);
    while (__builtin_amdgcn_s_memrealtime() - t0 < 3000) __builtin_amdgcn_s_sleep(2);
    const unsigned nk = min(__hip_atomic_fetch_or(&kcnt[lane], 0x10000u, RLX_WG), (unsigned)NCH);
    __builtin_amdgcn_raw_buffer_store_b64(__builtin_bit_cast(u32x2, epoch << 16 | nk), rw, lane == 0 ? FLOFF + split * 8 : OOB, 0, 16);
    unsigned long long f = 0;
    for (int spin = 0; spin < (1 << 20); ++spin) {
      f = __builtin_bit_cast(unsigned long long, __builtin_amdgcn_raw_buffer_load_b64(rw, lane < NSPLIT ? FLOFF + lane * 8 : OOB, 0, 16));
      if (__all(lane >= NSPLIT || (f >> 16) == epoch)) break;
      __builtin_amdgcn_s_sleep(32);
      asm volatile("" ::: "memory");
    }
    int left[NSPLIT], ndyn = 0;
#pragma unroll
    for (int j = 0; j < NSPLIT; ++j) { left[j] = ndyn; ndyn += REG - 2 * min(__builtin_amdgcn_readlane((int)f, j) & 0xffff, NCH); }
    while (__builtin_amdgcn_readfirstlane(__hip_atomic_load(&go, RLX_WG)) == 0) __builtin_amdgcn_s_sleep(8);
    {
      unsigned zero = 0;
      asm volatile("" : "+v"(zero));
      gu64* const wq = (gu64*)(gws + QOFF) + zero;
      int filled = 0, hprev = 0, pos = __builtin_amdgcn_readfirstlane(__builtin_amdgcn_raw_buffer_load_b32(rw, QOFF, 0, 16)) & 0xffff;
      for (;;) {
        const int h = __builtin_amdgcn_readfirstlane(__hip_atomic_load(&head[0], RLX_WG));
        int need = 0, base = ndyn;
        if (pos < ndyn) {
          const int rest = ndyn - pos - (h - hprev) * 7;
          const int tgt = max(AMIN, min(AMAX, rest * 2 / (NSPLIT * 3)));
          need = tgt - (filled - h);
          if (filled - h > min(LOW, tgt >> 1)) { __builtin_amdgcn_s_sleep(2); continue; }
          unsigned long long got = 0;
          if (lane == 0) got = __hip_atomic_fetch_add(wq, (unsigned long long)need, RLX_AGENT);
          asm volatile("" :: "v"(got));
          base = __builtin_amdgcn_readfirstlane((unsigned)got) & 0xffff;
        }
        const bool last = base + need >= ndyn;
        const int x = base + lane;
        int u = x + REG - left[1];
#pragma unroll
        for (int j = 1; j < NSPLIT; ++j) u = x >= left[j] ? x + (j + 1) * REG - (j + 1 < NSPLIT ? left[j + 1 < NSPLIT ? j + 1 : 0] : ndyn) : u;
        if (lane < need || last) __hip_atomic_store(&tick[(filled + lane) % NTICK], lane < need && x < ndyn ? u : -1, RLX_WG);
        filled += need;
        __hip_atomic_store(&avail, last ? 0x7fffffff : filled, RLX_WG);
        pos = base + need;
        hprev = h;
        if (last) break;
      }
    }

#define SWEEP(B)                                                                                                   \
    asm volatile("buffer_load_dwordx4 a[%c3:%c3+3], %0, %2, 0 offen sc1\n\t"                                       \
                 "buffer_load_dwordx4 a[%c3+4:%c3+7], %0, %2, 0 offen offset:1024 sc1\n\t"                         \
                 "buffer_load_dwordx4 a[%c3+8:%c3+11], %0, %2, 0 offen offset:2048 sc1\n\t"                        \
                 "buffer_load_dwordx4 a[%c3+12:%c3+15], %0, %2, 0 offen offset:3072 sc1\n\t"                       \
                 "buffer_load_dwordx4 a[%c3+16:%c3+19], %1, %2, 0 offen sc1"                                       \
                 :: "v"(rdo), "v"(rml), "s"(rw), "i"(B) : "memory", AGPRS)
#define RD10(B, x)                                                                                                 \
    asm volatile("v_accvgpr_read_b32 %0, a[%c10]\n\tv_accvgpr_read_b32 %1, a[%c10+2]\n\t"                          \
                 "v_accvgpr_read_b32 %2, a[%c10+4]\n\tv_accvgpr_read_b32 %3, a[%c10+6]\n\t"                        \
                 "v_accvgpr_read_b32 %4, a[%c10+8]\n\tv_accvgpr_read_b32 %5, a[%c10+10]\n\t"                       \
                 "v_accvgpr_read_b32 %6, a[%c10+12]\n\tv_accvgpr_read_b32 %7, a[%c10+14]\n\t"                      \
                 "v_accvgpr_read_b32 %8, a[%c10+16]\n\tv_accvgpr_read_b32 %9, a[%c10+18]"                          \
                 : "=v"(x[0]), "=v"(x[1]), "=v"(x[2]), "=v"(x[3]), "=v"(x[4]), "=v"(x[5]), "=v"(x[6]), "=v"(x[7]), \
                   "=v"(x[8]), "=v"(x[9]) : "i"(B) : "memory")
    unsigned hi[10], lo[10];
    asm volatile("s_waitcnt vmcnt(0)" ::: "memory");
    auto round = [&](auto n) __attribute__((always_inline)) {
      asm volatile("s_waitcnt vmcnt(%c0)" :: "i"(5 * (NPOLL - 1)) : "memory");
      RD10(n() * 20, lo);
      unsigned bad = 0;
#pragma unroll
      for (int i = 0; i < 10; ++i) { lo[i] ^= want[i]; bad |= lo[i]; }
      if (__all((bad & 1u) == 0)) { RD10(n() * 20 + 1, hi); return true; }
      SWEEP(n() * 20);
      return false;
    };
    SWEEP(0);
    if constexpr (NPOLL > 1) SWEEP(20);
    if constexpr (NPOLL > 2) SWEEP(40);
    if constexpr (NPOLL > 3) SWEEP(60);
    if constexpr (NPOLL > 4) SWEEP(80);
    for (int spin = 0; spin < (1 << 20); ++spin) {
      if (round(IC<0>{})) break;
      if constexpr (NPOLL > 1) if (round(IC<1>{})) break;
      if constexpr (NPOLL > 2) if (round(IC<2>{})) break;
      if constexpr (NPOLL > 3) if (round(IC<3>{})) break;
      if constexpr (NPOLL > 4) if (round(IC<4>{})) break;
    }
    const float mp = lane < NSPLIT ? (float)mkd(lo[9], hi[9]) : -INFINITY;
    float Mx = fmaxf(mp, dpp<XOR1>(mp));
    Mx = fmaxf(Mx, dpp<XOR2>(Mx));
    Mx = fmaxf(Mx, dpp<XOR7>(Mx));
    const int ep = expo(mp - ((Mx == -INFINITY) ? 0.f : Mx));
    double ls = __builtin_amdgcn_ldexp(mkd(lo[8], hi[8]), ep);
    ls += dpp<XOR1>(ls); ls += dpp<XOR2>(ls); ls += dpp<XOR7>(ls);
    double a0 = 0.0, a1 = 0.0;
#pragma unroll
    for (int i = 0; i < 4; ++i) {
      const int e = lane < 32 ? __builtin_amdgcn_readlane(ep, 2 * i) : __builtin_amdgcn_readlane(ep, 2 * i + 1);
      a0 += __builtin_amdgcn_ldexp(mkd(lo[2 * i], hi[2 * i]), e);
      a1 += __builtin_amdgcn_ldexp(mkd(lo[2 * i + 1], hi[2 * i + 1]), e);
    }
    const int le = -__builtin_amdgcn_frexp_exp(rdlane(ls, 0));
    const float lf = (float)__builtin_amdgcn_ldexp(rdlane(ls, 0), le);
    const __bf16 res = (__bf16)((float)__builtin_amdgcn_ldexp(swap32_sum(a0, a1), le) / lf);
    out[(grp * 2 + (split >> 2)) * D + (lane & 31) * 8 + (split & 3) * 2 + (lane >> 5)] = __builtin_bit_cast(unsigned short, res);
    __builtin_amdgcn_raw_buffer_store_b64(__builtin_bit_cast(u32x2, epoch), rw, split == 0 && lane == 0 ? EOFF : OOB, 0, 16);
    return;
  }

  const int r = lane >> 3, c = lane & 7;
  const unsigned kvo = r * 512 + c * 16, vvo = (lane >> 5) * 2048 + (lane & 31) * 16;
  u32x4 buf[2][4], qf[2][4];
  struct Req { __amdgpu_buffer_rsrc_t rs; unsigned vo; int so, st; };
  auto req = [&](bool isk, int unit) __attribute__((always_inline)) {
    return Req{__builtin_amdgcn_make_buffer_rsrc((void*)(isk ? kg : vg), 0, unit >= 0 ? SK * D * 2 : 0, 0x00020000),
               isk ? kvo : vvo, (unit >= 0 ? unit : 0) * UBYTES, isk ? 128 : 512};
  };
  auto fetch = [&](const Req& n, int j) __attribute__((always_inline)) {
    return __builtin_amdgcn_raw_buffer_load_b128(n.rs, n.vo, n.so + j * n.st, 2);
  };
  auto fetchv = [&](const Req& n, int j) __attribute__((always_inline)) {
    return __builtin_amdgcn_raw_buffer_load_b128(n.rs, n.vo, n.so + j * n.st, 2);
  };
  const int u0 = split * REG;
  {
    const Req a = req(true, u0 + 2 * wave), b = req(true, u0 + 2 * wave + 1);
#pragma unroll
    for (int j = 0; j < 4; ++j) { buf[0][j] = fetch(a, j); PIN(); }
#pragma unroll
    for (int g = 0; g < 2; ++g)
#pragma unroll
      for (int i = 0; i < 4; ++i) { qf[g][i] = q[(grp * 2 + g) * 32 + 8 * i + c]; PIN(); }
#pragma unroll
    for (int j = 0; j < 4; ++j) { buf[1][j] = fetch(b, j); PIN(); }
  }
  lds_barrier();
  PIN();

  auto scores = [&](u32x4 (&b)[4], const Req& n) __attribute__((always_inline)) {
    float d0 = 0.f, d1 = 0.f;
    asm volatile("" : "+v"(d0), "+v"(d1));
#pragma unroll
    for (int i = 0; i < 4; ++i) {
      const u32x4 kk = b[i];
#pragma unroll
      for (int e = 0; e < 4; ++e) { d0 = dot2(kk[e], qf[0][i][e], d0); d1 = dot2(kk[e], qf[1][i][e], d1); }
      asm volatile("" : "+v"(d0), "+v"(d1));
      PIN();
      b[i] = fetch(n, i);
      PIN();
    }
    asm volatile("" : "+v"(d0), "+v"(d1));
    const float t1 = d0 + dpp<XOR7>(d0), t2 = d1 + dpp<XOR7>(d1);
    float s = (c & 4) ? t2 : t1;
    s += dpp<XOR2>(s);
    return (s + dpp<XOR1>(s)) * C2;
  };
  float R = -INFINITY;
  double L = 0.0, A[8];
#pragma unroll
  for (int i = 0; i < 8; ++i) A[i] = 0.0;
  float bm;
  auto weigh = [&](float s, bool sums) __attribute__((always_inline)) {
    bm = __builtin_ceilf(key_max(s));
    const float p = __builtin_amdgcn_exp2f(s - ((bm == -INFINITY) ? 0.f : bm));
    const float Rn = fmaxf(R, 512.f * __builtin_rintf(bm * (1.f / 512.f)));
    if (__any(Rn != R)) {
      const double wa = pow2d(R - Rn), w0 = rdlane(wa, 0), w1 = rdlane(wa, 4), wh = lane < 32 ? w0 : w1;
      L *= wa;
      if (sums)
#pragma unroll
        for (int i = 0; i < 8; ++i) A[i] *= wh;
      R = Rn;
    }
    L += __builtin_amdgcn_ldexp((double)key_sum(p), expo(bm - R));
    return p;
  };
  unsigned tki = 0, tkv = 0, tka = 0;
  auto values = [&](u32x4 (&b)[4], const Req& n, auto wts, int e, bool draw) __attribute__((always_inline)) {
    f32x2 acc[2][4];
    auto pv = [&](int j, f32x2 p) __attribute__((always_inline)) {
      const u32x4 vv = b[j];
#pragma unroll
      for (int e = 0; e < 4; ++e) {
        const f32x2 vf = f32x2{__builtin_bit_cast(float, vv[e] << 16), __builtin_bit_cast(float, vv[e] & 0xffff0000u)};
        if (j == 0) { acc[0][e] = vf * p[0]; acc[1][e] = vf * p[1]; }
        else {
          acc[0][e] = __builtin_elementwise_fma(vf, f32x2{p[0], p[0]}, acc[0][e]);
          acc[1][e] = __builtin_elementwise_fma(vf, f32x2{p[1], p[1]}, acc[1][e]);
        }
      }
      asm volatile("" : "+v"(acc[0][0]), "+v"(acc[0][1]), "+v"(acc[0][2]), "+v"(acc[0][3]), "+v"(acc[1][0]), "+v"(acc[1][1]), "+v"(acc[1][2]), "+v"(acc[1][3]));
      PIN();
      b[j] = fetchv(n, j);
      PIN();
    };
    pv(0, wts(IC<0>{}));
    pv(1, wts(IC<1>{}));
    if (draw) tki = __hip_atomic_fetch_add(&head[lane], 1u, RLX_WG);
    PIN();
    pv(2, wts(IC<2>{}));
    if (draw) { tki = __builtin_amdgcn_readfirstlane(tki); tka = __hip_atomic_load(&avail, RLX_WG); tkv = __hip_atomic_load(&tick[tki % NTICK], RLX_WG); }
    PIN();
    pv(3, wts(IC<3>{}));
    const double w = __builtin_amdgcn_ldexp(1.0, e);
#pragma unroll
    for (int e = 0; e < 4; ++e) {
      const f32x2 t = swap32_sum(acc[0][e], acc[1][e]);
      A[2 * e] = __builtin_fma((double)t[0], w, A[2 * e]);
      A[2 * e + 1] = __builtin_fma((double)t[1], w, A[2 * e + 1]);
    }
    asm volatile("" : "+v"(A[0]), "+v"(A[1]), "+v"(A[2]), "+v"(A[3]), "+v"(A[4]), "+v"(A[5]), "+v"(A[6]), "+v"(A[7]));
  };
  auto ticket = [&]() __attribute__((always_inline)) {
    while (tki >= __builtin_amdgcn_readfirstlane(tka)) {
      __builtin_amdgcn_s_sleep(1);
      tka = __hip_atomic_load(&avail, RLX_WG);
      tkv = __hip_atomic_load(&tick[tki % NTICK], RLX_WG);
    }
    return (int)__builtin_amdgcn_readfirstlane(tkv);
  };

  int list = 0, nl = 0;
  {
    int cur = wave;
    unsigned nxt = NWAVE + wave;
#pragma unroll 1
    for (;;) {
      list = lane == nl ? cur : list;
      ++nl;
      const bool isk = nxt < NCH;
      const int nu = u0 + 2 * (isk ? (int)nxt : wave);
      const Req a = req(isk, nu), b = req(isk, nu + 1);
      PIN();
      float* const dst = &pw[2 * cur][(c & 3) ? 16 + (c >> 2) : 2 * r + (c >> 2)];
      const float pa = weigh(scores(buf[0], a), false);
      dst[0] = (c & 3) ? bm : pa;
      const unsigned t = __hip_atomic_fetch_add(&kcnt[lane], 1u, RLX_WG);
      asm volatile("" : "+v"(L), "+v"(R));
      PIN();
      const float pb = weigh(scores(buf[1], b), false);
      dst[20] = (c & 3) ? bm : pb;
      if (!isk) break;
      cur = nxt;
      nxt = __builtin_amdgcn_readfirstlane(t);
    }
  }
  int nxt;
  {
    struct Wt { f32x4 p[2]; float m; };
    auto wload = [&](int u) __attribute__((always_inline)) {
      const f32x4* s = (const f32x4*)&pw[u][(lane >> 5) * 8];
      return Wt{{s[0], s[1]}, pw[u][16 + (lane >> 5)]};
    };
    const float R0 = __builtin_bit_cast(float, __builtin_amdgcn_readlane(__builtin_bit_cast(int, R), 0)),
                R1 = __builtin_bit_cast(float, __builtin_amdgcn_readlane(__builtin_bit_cast(int, R), 4)), Rh = lane < 32 ? R0 : R1;
    Wt wa = wload(2 * wave);
    int pc = 2 * wave;
#pragma unroll 1
    for (int j = 1;; ++j) {
      if (j + 1 + 3 >= nl) __hip_atomic_store(&go, 1u, RLX_WG);
      const bool more = j < nl;
      int pj = 0;
      if (more) { pj = 2 * __builtin_amdgcn_readlane(list, j); nxt = u0 + pj; }
      else nxt = ticket();
      const Req a = req(!more, nxt), b = req(false, more ? nxt + 1 : nxt);
      const Wt wb = wload(pc + 1);
      PIN();
      values(buf[0], a, [&](auto n) __attribute__((always_inline)) { return f32x2{wa.p[n() >> 1][(n() & 1) * 2], wa.p[n() >> 1][(n() & 1) * 2 + 1]}; },
             expo(wa.m - Rh), false);
      wa = wload(pj);
      PIN();
      values(buf[1], b, [&](auto n) __attribute__((always_inline)) { return f32x2{wb.p[n() >> 1][(n() & 1) * 2], wb.p[n() >> 1][(n() & 1) * 2 + 1]}; },
             expo(wb.m - Rh), j + 1 >= nl);
      pc = pj;
      if (!more) break;
    }
  }
#pragma unroll 1
  while (nxt >= 0) {
    nxt = ticket();
    const Req a = req(true, nxt), b = req(false, nxt);
    PIN();
    const float p = weigh(scores(buf[0], a), true);
    const auto pr = __builtin_amdgcn_permlane16_swap(__float_as_uint(p), __float_as_uint(p), false, false);
    const int e = expo(bm - R), e0 = __builtin_amdgcn_readlane(e, 0), e1 = __builtin_amdgcn_readlane(e, 4);
    values(buf[1], b, [&](auto n) __attribute__((always_inline)) {
             const float x = __uint_as_float(pr[n() >> 1]);
             return f32x2{dpp<BCAST + (n() & 1) * 8>(x), dpp<BCAST + (n() & 1) * 8 + 4>(x)}; },
           lane < 32 ? e0 : e1, true);
  }

  {
    const unsigned mk = mark[lane];
    if (lane == 0 || lane == 4) { mrec[lane >> 2][wave] = R; lrec[lane >> 2][wave] = L; }
#pragma unroll
    for (int i = 0; i < 4; ++i) rec[wave][i][lane] = f64x2{A[2 * i], A[2 * i + 1]};
    asm volatile("" ::: "memory");
    const unsigned nth = __hip_atomic_fetch_add(&fin[lane], 1u, RLX_WG);
    asm volatile("" ::: "memory");
    float mm[NWAVE];
    double ll[NWAVE];
    f64x2 ov[NWAVE][4];
#pragma unroll
    for (int w = 0; w < NWAVE; ++w) { mm[w] = mrec[lane >> 5][w]; ll[w] = lrec[lane >> 5][w]; }
#pragma unroll
    for (int i = 0; i < 4; ++i)
#pragma unroll
      for (int w = 0; w < NWAVE; ++w) ov[w][i] = rec[w][i][lane];
    unsigned mb[10];
#pragma unroll
    for (int i = 0; i < 10; ++i) mb[i] = (mk >> i) & 1u;
    PIN();
    if (__builtin_amdgcn_readfirstlane(nth) == NWAVE - 1) {
      float Mw = mm[0];
#pragma unroll
      for (int w = 1; w < NWAVE; ++w) Mw = fmaxf(Mw, mm[w]);
      const float Ms = (Mw == -INFINITY) ? 0.f : Mw;
      double wt[NWAVE], ls = 0.0;
#pragma unroll
      for (int w = 0; w < NWAVE; ++w) { wt[w] = pow2d(mm[w] - Ms); ls = __builtin_fma(ll[w], wt[w], ls); }
      __builtin_amdgcn_raw_buffer_store_b128(pack2(ls, (double)Mw, mb[8], mb[9]), rw, pubml, 0, 16);
#pragma unroll
      for (int i = 0; i < 4; ++i) {
        double a0 = 0.0, a1 = 0.0;
#pragma unroll
        for (int w = 0; w < NWAVE; ++w) { a0 = __builtin_fma(ov[w][i][0], wt[w], a0); a1 = __builtin_fma(ov[w][i][1], wt[w], a1); }
        __builtin_amdgcn_raw_buffer_store_b128(pack2(a0, a1, mb[2 * i], mb[2 * i + 1]), rw, pub + i * NSPLIT * 512, 0, 16);
        PIN();
      }
    }
  }
  asm volatile("" :: "v"(buf[0][0]), "v"(buf[0][1]), "v"(buf[0][2]), "v"(buf[0][3]), "v"(buf[1][0]), "v"(buf[1][1]), "v"(buf[1][2]), "v"(buf[1][3]));
}

extern "C" void kernel_launch(void* const* d_in, const int* in_sizes, int n_in, void* d_out, int out_size, void* d_ws, size_t ws_size,
                              hipStream_t stream) {
  attn_decode<<<NGRP * NSPLIT, NTHR, 0, stream>>>((const u32x4*)d_in[0], (const char*)d_in[1], (const char*)d_in[2], (char*)d_ws,
                                                  (unsigned short*)d_out);
}
```

```cpp
#include <hip/hip_runtime.h>
#include <hip/hip_bf16.h>
#include <stdint.h>
#pragma clang fp contract(off)

typedef __attribute__((ext_vector_type(4))) unsigned u32x4;
typedef __attribute__((ext_vector_type(4))) float f32x4;
typedef __attribute__((ext_vector_type(2))) float f32x2;
typedef __attribute__((ext_vector_type(2))) double f64x2;
typedef __attribute__((ext_vector_type(2))) unsigned u32x2;
typedef __attribute__((ext_vector_type(2))) __bf16 bf16x2;
typedef __attribute__((address_space(1))) unsigned long long gu64;

constexpr int AMIN = 4, AMAX = 48, LOW = 6;

constexpr int BATCH = 4, HKV = 8, SK = 16384, D = 256;
constexpr int NGRP = BATCH * HKV;
constexpr int NWAVE = 4, NSPLIT = 8, NTHR = (NWAVE + 1) * 64;
constexpr int UBYTES = 8 * D * 2;
constexpr int REG = SK / (8 * NSPLIT), NCH = REG / 2;
constexpr int NTICK = NSPLIT * REG;
constexpr int NPOLL = 4;
constexpr int MLOFF = NSPLIT * NSPLIT * 512;
constexpr int FLOFF = MLOFF + 256;
constexpr int QOFF = FLOFF + 128, EOFF = QOFF + 128;
constexpr int GRPWS = EOFF + 128;
static_assert(AMAX + 3 * NWAVE <= 64 && AMIN >= 1, "tickets");
constexpr float C2 = 0.0625f * 1.4426950408889634f;
constexpr unsigned OOB = 0x40000000u;

__device__ __forceinline__ float dot2(unsigned k, unsigned q, float acc) {
  return __builtin_amdgcn_fdot2_f32_bf16(__builtin_bit_cast(bf16x2, k), __builtin_bit_cast(bf16x2, q), acc, false);
}
template <int CTRLW> __device__ __forceinline__ float dpp(float x) {
  return __builtin_bit_cast(float, __builtin_amdgcn_mov_dpp(__builtin_bit_cast(int, x), CTRLW, 0xf, 0xf, true));
}
template <int CTRLW> __device__ __forceinline__ double dpp(double x) {
  const u32x2 b = __builtin_bit_cast(u32x2, x);
  return __builtin_bit_cast(double, u32x2{(unsigned)__builtin_amdgcn_mov_dpp((int)b[0], CTRLW, 0xf, 0xf, true),
                                          (unsigned)__builtin_amdgcn_mov_dpp((int)b[1], CTRLW, 0xf, 0xf, true)});
}
constexpr int XOR1 = 0xB1, XOR2 = 0x4E, XOR7 = 0x141, XOR8 = 0x128, BCAST = 0x150;
__device__ __forceinline__ float key_max(float x) {
  x = fmaxf(x, dpp<XOR8>(x));
  auto s = __builtin_amdgcn_permlane16_swap(__float_as_uint(x), __float_as_uint(x), false, false);
  x = fmaxf(__uint_as_float(s[0]), __uint_as_float(s[1]));
  auto t = __builtin_amdgcn_permlane32_swap(__float_as_uint(x), __float_as_uint(x), false, false);
  return fmaxf(__uint_as_float(t[0]), __uint_as_float(t[1]));
}
__device__ __forceinline__ float key_sum(float x) {
  x += dpp<XOR8>(x);
  auto s = __builtin_amdgcn_permlane16_swap(__float_as_uint(x), __float_as_uint(x), false, false);
  x = __uint_as_float(s[0]) + __uint_as_float(s[1]);
  auto t = __builtin_amdgcn_permlane32_swap(__float_as_uint(x), __float_as_uint(x), false, false);
  return __uint_as_float(t[0]) + __uint_as_float(t[1]);
}
__device__ __forceinline__ double swap32_sum(double a, double b) {
  const u32x2 x = __builtin_bit_cast(u32x2, a), y = __builtin_bit_cast(u32x2, b);
  auto lo = __builtin_amdgcn_permlane32_swap(x[0], y[0], false, false);
  auto hi = __builtin_amdgcn_permlane32_swap(x[1], y[1], false, false);
  return __builtin_bit_cast(double, u32x2{lo[0], hi[0]}) + __builtin_bit_cast(double, u32x2{lo[1], hi[1]});
}
__device__ __forceinline__ f32x2 swap32_sum(f32x2 a, f32x2 b) {
  const u32x2 x = __builtin_bit_cast(u32x2, a), y = __builtin_bit_cast(u32x2, b);
  auto lo = __builtin_amdgcn_permlane32_swap(x[0], y[0], false, false);
  auto hi = __builtin_amdgcn_permlane32_swap(x[1], y[1], false, false);
  return __builtin_bit_cast(f32x2, u32x2{lo[0], hi[0]}) + __builtin_bit_cast(f32x2, u32x2{lo[1], hi[1]});
}
__device__ __forceinline__ double rdlane(double x, int l) {
  const u32x2 b = __builtin_bit_cast(u32x2, x);
  return __builtin_bit_cast(double, u32x2{(unsigned)__builtin_amdgcn_readlane((int)b[0], l), (unsigned)__builtin_amdgcn_readlane((int)b[1], l)});
}
__device__ __forceinline__ int expo(float d) { return (int)fmaxf(d, -4000.f); }
__device__ __forceinline__ double pow2d(float d) { return __builtin_amdgcn_ldexp(1.0, expo(d)); }
__device__ __forceinline__ u32x4 pack2(double x, double y, unsigned mx, unsigned my) {
  const u32x2 a = __builtin_bit_cast(u32x2, x), b = __builtin_bit_cast(u32x2, y);
  return u32x4{(a[0] & ~1u) | mx, a[1], (b[0] & ~1u) | my, b[1]};
}
__device__ __forceinline__ double mkd(unsigned lo, unsigned hi) { return __builtin_bit_cast(double, u32x2{lo, hi}); }
__device__ __forceinline__ void lds_barrier() {
  asm volatile("s_waitcnt lgkmcnt(0)" ::: "memory");
  __builtin_amdgcn_s_barrier();
  asm volatile("" ::: "memory");
}
template <int N> struct IC { constexpr int operator()() const { return N; } };
#define A10(x) "a" #x "0", "a" #x "1", "a" #x "2", "a" #x "3", "a" #x "4", "a" #x "5", "a" #x "6", "a" #x "7", "a" #x "8", "a" #x "9"
#define AGPRS A10(), A10(1), A10(2), A10(3), A10(4), A10(5), A10(6), A10(7), A10(8), A10(9)
#define PIN() __builtin_amdgcn_sched_barrier(0)
#define RLX_AGENT __ATOMIC_RELAXED, __HIP_MEMORY_SCOPE_AGENT
#define RLX_WG __ATOMIC_RELAXED, __HIP_MEMORY_SCOPE_WORKGROUP

__global__ void __launch_bounds__(NTHR, 1) attn_decode(const u32x4* __restrict__ q, const char* __restrict__ k,
                                                       const char* __restrict__ v, char* __restrict__ ws,
                                                       unsigned short* __restrict__ out) {
  __shared__ __attribute__((aligned(16))) float pw[REG][20];
  __shared__ __attribute__((aligned(16))) f64x2 rec[NWAVE][4][64];
  __shared__ __attribute__((aligned(16))) double lrec[2][NWAVE];
  __shared__ __attribute__((aligned(16))) float mrec[2][NWAVE];
  __shared__ unsigned tick[NTICK];
  __shared__ unsigned kcnt[64];
  __shared__ unsigned head[64];
  __shared__ unsigned fin[64];
  __shared__ unsigned mark[64];
  __shared__ unsigned go, avail;
  asm volatile("" :: "s"(q), "s"(k), "s"(v), "s"(ws), "s"(out));
  const int tid = threadIdx.x, lane = tid & 63, wave = __builtin_amdgcn_readfirstlane(tid >> 6);
  const int grp = blockIdx.x / NSPLIT, split = blockIdx.x % NSPLIT;
  char* const gws = ws + (size_t)grp * GRPWS;
  const auto rw = __builtin_amdgcn_make_buffer_rsrc((void*)gws, 0, GRPWS, 0x00020000);
  const char* kg = k + (size_t)grp * SK * D * 2;
  const char* vg = v + (size_t)grp * SK * D * 2;

  const unsigned pub = ((lane >> 5) * 4 * NSPLIT + split) * 512 + (lane & 31) * 16;
  const unsigned pubml = (lane & 31) == 0 ? MLOFF + (lane >> 5) * 128 + split * 16 : OOB;

  if (wave == NWAVE) {
    const unsigned long long t0 = __builtin_amdgcn_s_memrealtime();
    const unsigned rdo = split * NSPLIT * 512 + lane * 16, rml = lane < NSPLIT ? MLOFF + (split >> 2) * 128 + lane * 16 : OOB;
    __builtin_amdgcn_s_setprio(3);
    kcnt[lane] = 2 * NWAVE;
    head[lane] = 0;
    fin[lane] = 0;
    go = 0;
    avail = 0;
    lds_barrier();
    const u32x2 e_in = __builtin_amdgcn_raw_buffer_load_b64(rw, EOFF, 0, 16);
    u32x4 ow[5], orr[5];
#pragma unroll
    for (int i = 0; i < 4; ++i) {
      ow[i] = __builtin_amdgcn_raw_buffer_load_b128(rw, pub + i * NSPLIT * 512, 0, 16);
      orr[i] = __builtin_amdgcn_raw_buffer_load_b128(rw, rdo + i * 1024, 0, 16);
    }
    ow[4] = __builtin_amdgcn_raw_buffer_load_b128(rw, pubml, 0, 16);
    orr[4] = __builtin_amdgcn_raw_buffer_load_b128(rw, rml, 0, 16);
    PIN();
    const unsigned long long epoch = (__builtin_bit_cast(unsigned long long, e_in) + 1) & 0xffffffffffffull;
    __builtin_amdgcn_raw_buffer_store_b64(__builtin_bit_cast(u32x2, epoch << 16), rw, split == 0 && lane == 0 ? QOFF : OOB, 0, 16);
    unsigned mk = 0, want[10];
#pragma unroll
    for (int i = 0; i < 10; ++i) {
      mk |= (~ow[i >> 1][(i & 1) * 2] & 1u) << i;
      want[i] = (i < 8 || lane < NSPLIT) ? ~orr[i >> 1][(i & 1) * 2] & 1u : 0u;
    }
    mark[lane] = mk;
    asm volatile("s_waitcnt vmcnt(0)" :: "v"(want[0]), "v"(want[1]), "v"(want[2]), "v"(want[3]), "v"(want[4]), "v"(want[5]),
                 "v"(want[6]), "v"(want[7]), "v"(want[8]), "v"(want[9]) : "memory");
    while (__builtin_amdgcn_s_memrealtime() - t0 < 3100 - 100) __builtin_amdgcn_s_sleep(32);
    while (__builtin_amdgcn_s_memrealtime() - t0 < 3100) __builtin_amdgcn_s_sleep(2);
    const unsigned nk = min(__hip_atomic_fetch_or(&kcnt[lane], 0x10000u, RLX_WG), (unsigned)NCH);
    __builtin_amdgcn_raw_buffer_store_b64(__builtin_bit_cast(u32x2, epoch << 16 | nk), rw, lane == 0 ? FLOFF + split * 8 : OOB, 0, 16);
    unsigned long long f = 0;
    for (int spin = 0; spin < (1 << 20); ++spin) {
      f = __builtin_bit_cast(unsigned long long, __builtin_amdgcn_raw_buffer_load_b64(rw, lane < NSPLIT ? FLOFF + lane * 8 : OOB, 0, 16));
      if (__all(lane >= NSPLIT || (f >> 16) == epoch)) break;
      __builtin_amdgcn_s_sleep(32);
      asm volatile("" ::: "memory");
    }
    int left[NSPLIT], ndyn = 0;
#pragma unroll
    for (int j = 0; j < NSPLIT; ++j) { left[j] = ndyn; ndyn += REG - 2 * min(__builtin_amdgcn_readlane((int)f, j) & 0xffff, NCH); }
    while (__builtin_amdgcn_readfirstlane(__hip_atomic_load(&go, RLX_WG)) == 0) __builtin_amdgcn_s_sleep(8);
    {
      unsigned zero = 0;
      asm volatile("" : "+v"(zero));
      gu64* const wq = (gu64*)(gws + QOFF) + zero;
      int filled = 0, hprev = 0, pos = __builtin_amdgcn_readfirstlane(__builtin_amdgcn_raw_buffer_load_b32(rw, QOFF, 0, 16)) & 0xffff;
      for (;;) {
        const int h = __builtin_amdgcn_readfirstlane(__hip_atomic_load(&head[0], RLX_WG));
        int need = 0, base = ndyn;
        if (pos < ndyn) {
          const int rest = ndyn - pos - (h - hprev) * 7;
          const int tgt = max(AMIN, min(AMAX, rest * 2 / (NSPLIT * 3)));
          need = tgt - (filled - h);
          if (filled - h > min(LOW, tgt >> 1)) { __builtin_amdgcn_s_sleep(2); continue; }
          unsigned long long got = 0;
          if (lane == 0) got = __hip_atomic_fetch_add(wq, (unsigned long long)need, RLX_AGENT);
          asm volatile("" :: "v"(got));
          base = __builtin_amdgcn_readfirstlane((unsigned)got) & 0xffff;
        }
        const bool last = base + need >= ndyn;
        const int x = base + lane;
        int u = x + REG - left[1];
#pragma unroll
        for (int j = 1; j < NSPLIT; ++j) u = x >= left[j] ? x + (j + 1) * REG - (j + 1 < NSPLIT ? left[j + 1 < NSPLIT ? j + 1 : 0] : ndyn) : u;
        if (lane < need || last) __hip_atomic_store(&tick[(filled + lane) % NTICK], lane < need && x < ndyn ? u : -1, RLX_WG);
        filled += need;
        __hip_atomic_store(&avail, last ? 0x7fffffff : filled, RLX_WG);
        pos = base + need;
        hprev = h;
        if (last) break;
      }
    }

#define SWEEP(B)                                                                                                   \
    asm volatile("buffer_load_dwordx4 a[%c3:%c3+3], %0, %2, 0 offen sc1\n\t"                                       \
                 "buffer_load_dwordx4 a[%c3+4:%c3+7], %0, %2, 0 offen offset:1024 sc1\n\t"                         \
                 "buffer_load_dwordx4 a[%c3+8:%c3+11], %0, %2, 0 offen offset:2048 sc1\n\t"                        \
                 "buffer_load_dwordx4 a[%c3+12:%c3+15], %0, %2, 0 offen offset:3072 sc1\n\t"                       \
                 "buffer_load_dwordx4 a[%c3+16:%c3+19], %1, %2, 0 offen sc1"                                       \
                 :: "v"(rdo), "v"(rml), "s"(rw), "i"(B) : "memory", AGPRS)
#define RD10(B, x)                                                                                                 \
    asm volatile("v_accvgpr_read_b32 %0, a[%c10]\n\tv_accvgpr_read_b32 %1, a[%c10+2]\n\t"                          \
                 "v_accvgpr_read_b32 %2, a[%c10+4]\n\tv_accvgpr_read_b32 %3, a[%c10+6]\n\t"                        \
                 "v_accvgpr_read_b32 %4, a[%c10+8]\n\tv_accvgpr_read_b32 %5, a[%c10+10]\n\t"                       \
                 "v_accvgpr_read_b32 %6, a[%c10+12]\n\tv_accvgpr_read_b32 %7, a[%c10+14]\n\t"                      \
                 "v_accvgpr_read_b32 %8, a[%c10+16]\n\tv_accvgpr_read_b32 %9, a[%c10+18]"                          \
                 : "=v"(x[0]), "=v"(x[1]), "=v"(x[2]), "=v"(x[3]), "=v"(x[4]), "=v"(x[5]), "=v"(x[6]), "=v"(x[7]), \
                   "=v"(x[8]), "=v"(x[9]) : "i"(B) : "memory")
    unsigned hi[10], lo[10];
    asm volatile("s_waitcnt vmcnt(0)" ::: "memory");
    auto round = [&](auto n) __attribute__((always_inline)) {
      asm volatile("s_waitcnt vmcnt(%c0)" :: "i"(5 * (NPOLL - 1)) : "memory");
      RD10(n() * 20, lo);
      unsigned bad = 0;
#pragma unroll
      for (int i = 0; i < 10; ++i) { lo[i] ^= want[i]; bad |= lo[i]; }
      if (__all((bad & 1u) == 0)) { RD10(n() * 20 + 1, hi); return true; }
      SWEEP(n() * 20);
      return false;
    };
    SWEEP(0);
    if constexpr (NPOLL > 1) SWEEP(20);
    if constexpr (NPOLL > 2) SWEEP(40);
    if constexpr (NPOLL > 3) SWEEP(60);
    if constexpr (NPOLL > 4) SWEEP(80);
    for (int spin = 0; spin < (1 << 20); ++spin) {
      if (round(IC<0>{})) break;
      if constexpr (NPOLL > 1) if (round(IC<1>{})) break;
      if constexpr (NPOLL > 2) if (round(IC<2>{})) break;
      if constexpr (NPOLL > 3) if (round(IC<3>{})) break;
      if constexpr (NPOLL > 4) if (round(IC<4>{})) break;
    }
    const float mp = lane < NSPLIT ? (float)mkd(lo[9], hi[9]) : -INFINITY;
    float Mx = fmaxf(mp, dpp<XOR1>(mp));
    Mx = fmaxf(Mx, dpp<XOR2>(Mx));
    Mx = fmaxf(Mx, dpp<XOR7>(Mx));
    const int ep = expo(mp - ((Mx == -INFINITY) ? 0.f : Mx));
    double ls = __builtin_amdgcn_ldexp(mkd(lo[8], hi[8]), ep);
    ls += dpp<XOR1>(ls); ls += dpp<XOR2>(ls); ls += dpp<XOR7>(ls);
    double a0 = 0.0, a1 = 0.0;
#pragma unroll
    for (int i = 0; i < 4; ++i) {
      const int e = lane < 32 ? __builtin_amdgcn_readlane(ep, 2 * i) : __builtin_amdgcn_readlane(ep, 2 * i + 1);
      a0 += __builtin_amdgcn_ldexp(mkd(lo[2 * i], hi[2 * i]), e);
      a1 += __builtin_amdgcn_ldexp(mkd(lo[2 * i + 1], hi[2 * i + 1]), e);
    }
    const int le = -__builtin_amdgcn_frexp_exp(rdlane(ls, 0));
    const float lf = (float)__builtin_amdgcn_ldexp(rdlane(ls, 0), le);
    const __bf16 res = (__bf16)((float)__builtin_amdgcn_ldexp(swap32_sum(a0, a1), le) / lf);
    out[(grp * 2 + (split >> 2)) * D + (lane & 31) * 8 + (split & 3) * 2 + (lane >> 5)] = __builtin_bit_cast(unsigned short, res);
    __builtin_amdgcn_raw_buffer_store_b64(__builtin_bit_cast(u32x2, epoch), rw, split == 0 && lane == 0 ? EOFF : OOB, 0, 16);
    return;
  }

  const int r = lane >> 3, c = lane & 7;
  const unsigned kvo = r * 512 + c * 16, vvo = (lane >> 5) * 2048 + (lane & 31) * 16;
  u32x4 buf[2][4], qf[2][4];
  struct Req { __amdgpu_buffer_rsrc_t rs; unsigned vo; int so, st; };
  auto req = [&](bool isk, int unit) __attribute__((always_inline)) {
    return Req{__builtin_amdgcn_make_buffer_rsrc((void*)(isk ? kg : vg), 0, unit >= 0 ? SK * D * 2 : 0, 0x00020000),
               isk ? kvo : vvo, (unit >= 0 ? unit : 0) * UBYTES, isk ? 128 : 512};
  };
  auto fetch = [&](const Req& n, int j) __attribute__((always_inline)) {
    return __builtin_amdgcn_raw_buffer_load_b128(n.rs, n.vo, n.so + j * n.st, 2);
  };
  auto fetchv = [&](const Req& n, int j) __attribute__((always_inline)) {
    return __builtin_amdgcn_raw_buffer_load_b128(n.rs, n.vo, n.so + j * n.st, 2);
  };
  const int u0 = split * REG;
  {
    const Req a = req(true, u0 + 2 * wave), b = req(true, u0 + 2 * wave + 1);
#pragma unroll
    for (int j = 0; j < 4; ++j) { buf[0][j] = fetch(a, j); PIN(); }
#pragma unroll
    for (int g = 0; g < 2; ++g)
#pragma unroll
      for (int i = 0; i < 4; ++i) { qf[g][i] = q[(grp * 2 + g) * 32 + 8 * i + c]; PIN(); }
#pragma unroll
    for (int j = 0; j < 4; ++j) { buf[1][j] = fetch(b, j); PIN(); }
  }
  lds_barrier();
  PIN();

  auto scores = [&](u32x4 (&b)[4], const Req& n) __attribute__((always_inline)) {
    float d0 = 0.f, d1 = 0.f;
    asm volatile("" : "+v"(d0), "+v"(d1));
#pragma unroll
    for (int i = 0; i < 4; ++i) {
      const u32x4 kk = b[i];
#pragma unroll
      for (int e = 0; e < 4; ++e) { d0 = dot2(kk[e], qf[0][i][e], d0); d1 = dot2(kk[e], qf[1][i][e], d1); }
      asm volatile("" : "+v"(d0), "+v"(d1));
      PIN();
      b[i] = fetch(n, i);
      PIN();
    }
    asm volatile("" : "+v"(d0), "+v"(d1));
    const float t1 = d0 + dpp<XOR7>(d0), t2 = d1 + dpp<XOR7>(d1);
    float s = (c & 4) ? t2 : t1;
    s += dpp<XOR2>(s);
    return (s + dpp<XOR1>(s)) * C2;
  };
  float R = -INFINITY;
  double L = 0.0, A[8];
#pragma unroll
  for (int i = 0; i < 8; ++i) A[i] = 0.0;
  float bm;
  auto weigh = [&](float s, bool sums) __attribute__((always_inline)) {
    bm = __builtin_ceilf(key_max(s));
    const float p = __builtin_amdgcn_exp2f(s - ((bm == -INFINITY) ? 0.f : bm));
    const float Rn = fmaxf(R, 512.f * __builtin_rintf(bm * (1.f / 512.f)));
    if (__any(Rn != R)) {
      const double wa = pow2d(R - Rn), w0 = rdlane(wa, 0), w1 = rdlane(wa, 4), wh = lane < 32 ? w0 : w1;
      L *= wa;
      if (sums)
#pragma unroll
        for (int i = 0; i < 8; ++i) A[i] *= wh;
      R = Rn;
    }
    L += __builtin_amdgcn_ldexp((double)key_sum(p), expo(bm - R));
    return p;
  };
  unsigned tki = 0, tkv = 0, tka = 0;
  auto values = [&](u32x4 (&b)[4], const Req& n, auto wts, int e, bool draw) __attribute__((always_inline)) {
    f32x2 acc[2][4];
    auto pv = [&](int j, f32x2 p) __attribute__((always_inline)) {
      const u32x4 vv = b[j];
#pragma unroll
      for (int e = 0; e < 4; ++e) {
        const f32x2 vf = f32x2{__builtin_bit_cast(float, vv[e] << 16), __builtin_bit_cast(float, vv[e] & 0xffff0000u)};
        if (j == 0) { acc[0][e] = vf * p[0]; acc[1][e] = vf * p[1]; }
        else {
          acc[0][e] = __builtin_elementwise_fma(vf, f32x2{p[0], p[0]}, acc[0][e]);
          acc[1][e] = __builtin_elementwise_fma(vf, f32x2{p[1], p[1]}, acc[1][e]);
        }
      }
      asm volatile("" : "+v"(acc[0][0]), "+v"(acc[0][1]), "+v"(acc[0][2]), "+v"(acc[0][3]), "+v"(acc[1][0]), "+v"(acc[1][1]), "+v"(acc[1][2]), "+v"(acc[1][3]));
      PIN();
      b[j] = fetchv(n, j);
      PIN();
    };
    pv(0, wts(IC<0>{}));
    pv(1, wts(IC<1>{}));
    if (draw) tki = __hip_atomic_fetch_add(&head[lane], 1u, RLX_WG);
    PIN();
    pv(2, wts(IC<2>{}));
    if (draw) { tki = __builtin_amdgcn_readfirstlane(tki); tka = __hip_atomic_load(&avail, RLX_WG); tkv = __hip_atomic_load(&tick[tki % NTICK], RLX_WG); }
    PIN();
    pv(3, wts(IC<3>{}));
    const double w = __builtin_amdgcn_ldexp(1.0, e);
#pragma unroll
    for (int e = 0; e < 4; ++e) {
      const f32x2 t = swap32_sum(acc[0][e], acc[1][e]);
      A[2 * e] = __builtin_fma((double)t[0], w, A[2 * e]);
      A[2 * e + 1] = __builtin_fma((double)t[1], w, A[2 * e + 1]);
    }
    asm volatile("" : "+v"(A[0]), "+v"(A[1]), "+v"(A[2]), "+v"(A[3]), "+v"(A[4]), "+v"(A[5]), "+v"(A[6]), "+v"(A[7]));
  };
  auto ticket = [&]() __attribute__((always_inline)) {
    while (tki >= __builtin_amdgcn_readfirstlane(tka)) {
      __builtin_amdgcn_s_sleep(1);
      tka = __hip_atomic_load(&avail, RLX_WG);
      tkv = __hip_atomic_load(&tick[tki % NTICK], RLX_WG);
    }
    return (int)__builtin_amdgcn_readfirstlane(tkv);
  };

  int list = 0, nl = 0;
  {
    int cur = wave;
    unsigned nxt = NWAVE + wave;
#pragma unroll 1
    for (;;) {
      list = lane == nl ? cur : list;
      ++nl;
      const bool isk = nxt < NCH, draw = nl < 62;
      const int nu = u0 + 2 * (isk ? (int)nxt : wave);
      const Req a = req(isk, nu), b = req(isk, nu + 1);
      PIN();
      float* const dst = &pw[2 * cur][(c & 3) ? 16 + (c >> 2) : 2 * r + (c >> 2)];
      const float pa = weigh(scores(buf[0], a), false);
      dst[0] = (c & 3) ? bm : pa;
      const unsigned t = __hip_atomic_fetch_add(&kcnt[lane], draw ? 1u : 0u, RLX_WG);
      asm volatile("" : "+v"(L), "+v"(R));
      PIN();
      const float pb = weigh(scores(buf[1], b), false);
      dst[20] = (c & 3) ? bm : pb;
      if (!isk) break;
      cur = nxt;
      nxt = __builtin_amdgcn_readfirstlane(t) | (draw ? 0 : NCH);
    }
  }
  int nxt;
  {
    struct Wt { f32x4 p[2]; float m; };
    auto wload = [&](int u) __attribute__((always_inline)) {
      const f32x4* s = (const f32x4*)&pw[u][(lane >> 5) * 8];
      return Wt{{s[0], s[1]}, pw[u][16 + (lane >> 5)]};
    };
    const float R0 = __builtin_bit_cast(float, __builtin_amdgcn_readlane(__builtin_bit_cast(int, R), 0)),
                R1 = __builtin_bit_cast(float, __builtin_amdgcn_readlane(__builtin_bit_cast(int, R), 4)), Rh = lane < 32 ? R0 : R1;
    Wt wa = wload(2 * wave);
    int pc = 2 * wave;
#pragma unroll 1
    for (int j = 1;; ++j) {
      if (j + 1 + 3 >= nl) __hip_atomic_store(&go, 1u, RLX_WG);
      const bool more = j < nl;
      int pj = 0;
      if (more) { pj = 2 * __builtin_amdgcn_readlane(list, j); nxt = u0 + pj; }
      else nxt = ticket();
      const Req a = req(!more, nxt), b = req(false, more ? nxt + 1 : nxt);
      const Wt wb = wload(pc + 1);
      PIN();
      values(buf[0], a, [&](auto n) __attribute__((always_inline)) { return f32x2{wa.p[n() >> 1][(n() & 1) * 2], wa.p[n() >> 1][(n() & 1) * 2 + 1]}; },
             expo(wa.m - Rh), false);
      wa = wload(pj);
      PIN();
      values(buf[1], b, [&](auto n) __attribute__((always_inline)) { return f32x2{wb.p[n() >> 1][(n() & 1) * 2], wb.p[n() >> 1][(n() & 1) * 2 + 1]}; },
             expo(wb.m - Rh), j + 1 >= nl);
      pc = pj;
      if (!more) break;
    }
  }
#pragma unroll 1
  while (nxt >= 0) {
    nxt = ticket();
    const Req a = req(true, nxt), b = req(false, nxt);
    PIN();
    const float p = weigh(scores(buf[0], a), true);
    const auto pr = __builtin_amdgcn_permlane16_swap(__float_as_uint(p), __float_as_uint(p), false, false);
    const int e = expo(bm - R), e0 = __builtin_amdgcn_readlane(e, 0), e1 = __builtin_amdgcn_readlane(e, 4);
    values(buf[1], b, [&](auto n) __attribute__((always_inline)) {
             const float x = __uint_as_float(pr[n() >> 1]);
             return f32x2{dpp<BCAST + (n() & 1) * 8>(x), dpp<BCAST + (n() & 1) * 8 + 4>(x)}; },
           lane < 32 ? e0 : e1, true);
  }

  {
    const unsigned mk = mark[lane];
    if (lane == 0 || lane == 4) { mrec[lane >> 2][wave] = R; lrec[lane >> 2][wave] = L; }
#pragma unroll
    for (int i = 0; i < 4; ++i) rec[wave][i][lane] = f64x2{A[2 * i], A[2 * i + 1]};
    asm volatile("" ::: "memory");
    const unsigned nth = __hip_atomic_fetch_add(&fin[lane], 1u, RLX_WG);
    asm volatile("" ::: "memory");
    float mm[NWAVE];
    double ll[NWAVE];
    f64x2 ov[NWAVE][4];
#pragma unroll
    for (int w = 0; w < NWAVE; ++w) { mm[w] = mrec[lane >> 5][w]; ll[w] = lrec[lane >> 5][w]; }
#pragma unroll
    for (int i = 0; i < 4; ++i)
#pragma unroll
      for (int w = 0; w < NWAVE; ++w) ov[w][i] = rec[w][i][lane];
    unsigned mb[10];
#pragma unroll
    for (int i = 0; i < 10; ++i) mb[i] = (mk >> i) & 1u;
    PIN();
    if (__builtin_amdgcn_readfirstlane(nth) == NWAVE - 1) {
      float Mw = mm[0];
#pragma unroll
      for (int w = 1; w < NWAVE; ++w) Mw = fmaxf(Mw, mm[w]);
      const float Ms = (Mw == -INFINITY) ? 0.f : Mw;
      double wt[NWAVE], ls = 0.0;
#pragma unroll
      for (int w = 0; w < NWAVE; ++w) { wt[w] = pow2d(mm[w] - Ms); ls = __builtin_fma(ll[w], wt[w], ls); }
      __builtin_amdgcn_raw_buffer_store_b128(pack2(ls, (double)Mw, mb[8], mb[9]), rw, pubml, 0, 16);
#pragma unroll
      for (int i = 0; i < 4; ++i) {
        double a0 = 0.0, a1 = 0.0;
#pragma unroll
        for (int w = 0; w < NWAVE; ++w) { a0 = __builtin_fma(ov[w][i][0], wt[w], a0); a1 = __builtin_fma(ov[w][i][1], wt[w], a1); }
        __builtin_amdgcn_raw_buffer_store_b128(pack2(a0, a1, mb[2 * i], mb[2 * i + 1]), rw, pub + i * NSPLIT * 512, 0, 16);
        PIN();
      }
    }
  }
  asm volatile("" :: "v"(buf[0][0]), "v"(buf[0][1]), "v"(buf[0][2]), "v"(buf[0][3]), "v"(buf[1][0]), "v"(buf[1][1]), "v"(buf[1][2]), "v"(buf[1][3]));
}

extern "C" void kernel_launch(void* const* d_in, const int* in_sizes, int n_in, void* d_out, int out_size, void* d_ws, size_t ws_size,
                              hipStream_t stream) {
  attn_decode<<<NGRP * NSPLIT, NTHR, 0, stream>>>((const u32x4*)d_in[0], (const char*)d_in[1], (const char*)d_in[2], (char*)d_ws,
                                                  (unsigned short*)d_out);
}
```

```cpp
#include <hip/hip_runtime.h>
#include <hip/hip_bf16.h>
#include <stdint.h>
#pragma clang fp contract(off)

typedef __attribute__((ext_vector_type(4))) unsigned u32x4;
typedef __attribute__((ext_vector_type(4))) float f32x4;
typedef __attribute__((ext_vector_type(2))) float f32x2;
typedef __attribute__((ext_vector_type(2))) double f64x2;
typedef __attribute__((ext_vector_type(2))) unsigned u32x2;
typedef __attribute__((ext_vector_type(2))) __bf16 bf16x2;


constexpr int BATCH = 4, HKV = 8, SK = 16384, D = 256;
constexpr int NGRP = BATCH * HKV;
constexpr int NWAVE = 4, NSPLIT = 8, NTHR = (NWAVE + 1) * 64;
constexpr int UBYTES = 8 * D * 2;
constexpr int REG = SK / (8 * NSPLIT), NCH = REG / 2;
constexpr int NTICK = 2 * NSPLIT * REG, SHUT = 0x10000;
constexpr int VPAIR = 0x10000;
constexpr int MLOFF = NSPLIT * NSPLIT * 512;
constexpr int FLOFF = MLOFF + 256;
constexpr int EOFF = FLOFF + 128;
constexpr int GRPWS = EOFF + 128;
constexpr float C2 = 0.0625f * 1.4426950408889634f;
constexpr unsigned OOB = 0x40000000u;

__device__ __forceinline__ float dot2(unsigned k, unsigned q, float acc) {
  return __builtin_amdgcn_fdot2_f32_bf16(__builtin_bit_cast(bf16x2, k), __builtin_bit_cast(bf16x2, q), acc, false);
}
template <int CTRLW> __device__ __forceinline__ float dpp(float x) {
  return __builtin_bit_cast(float, __builtin_amdgcn_mov_dpp(__builtin_bit_cast(int, x), CTRLW, 0xf, 0xf, true));
}
template <int CTRLW> __device__ __forceinline__ double dpp(double x) {
  const u32x2 b = __builtin_bit_cast(u32x2, x);
  return __builtin_bit_cast(double, u32x2{(unsigned)__builtin_amdgcn_mov_dpp((int)b[0], CTRLW, 0xf, 0xf, true),
                                          (unsigned)__builtin_amdgcn_mov_dpp((int)b[1], CTRLW, 0xf, 0xf, true)});
}
constexpr int QUAD = 0x55, XOR1 = 0xB1, XOR2 = 0x4E, XOR7 = 0x141, XOR8 = 0x128, BCAST = 0x150;
__device__ __forceinline__ float key_max(float x) {
  x = fmaxf(x, dpp<XOR8>(x));
  auto s = __builtin_amdgcn_permlane16_swap(__float_as_uint(x), __float_as_uint(x), false, false);
  x = fmaxf(__uint_as_float(s[0]), __uint_as_float(s[1]));
  auto t = __builtin_amdgcn_permlane32_swap(__float_as_uint(x), __float_as_uint(x), false, false);
  return fmaxf(__uint_as_float(t[0]), __uint_as_float(t[1]));
}
__device__ __forceinline__ float key_sum(float x) {
  x += dpp<XOR8>(x);
  auto s = __builtin_amdgcn_permlane16_swap(__float_as_uint(x), __float_as_uint(x), false, false);
  x = __uint_as_float(s[0]) + __uint_as_float(s[1]);
  auto t = __builtin_amdgcn_permlane32_swap(__float_as_uint(x), __float_as_uint(x), false, false);
  return __uint_as_float(t[0]) + __uint_as_float(t[1]);
}
__device__ __forceinline__ double swap32_sum(double a, double b) {
  const u32x2 x = __builtin_bit_cast(u32x2, a), y = __builtin_bit_cast(u32x2, b);
  auto lo = __builtin_amdgcn_permlane32_swap(x[0], y[0], false, false);
  auto hi = __builtin_amdgcn_permlane32_swap(x[1], y[1], false, false);
  return __builtin_bit_cast(double, u32x2{lo[0], hi[0]}) + __builtin_bit_cast(double, u32x2{lo[1], hi[1]});
}
__device__ __forceinline__ f32x2 swap32_sum(f32x2 a, f32x2 b) {
  const u32x2 x = __builtin_bit_cast(u32x2, a), y = __builtin_bit_cast(u32x2, b);
  auto lo = __builtin_amdgcn_permlane32_swap(x[0], y[0], false, false);
  auto hi = __builtin_amdgcn_permlane32_swap(x[1], y[1], false, false);
  return __builtin_bit_cast(f32x2, u32x2{lo[0], hi[0]}) + __builtin_bit_cast(f32x2, u32x2{lo[1], hi[1]});
}
__device__ __forceinline__ double rdlane(double x, int l) {
  const u32x2 b = __builtin_bit_cast(u32x2, x);
  return __builtin_bit_cast(double, u32x2{(unsigned)__builtin_amdgcn_readlane((int)b[0], l), (unsigned)__builtin_amdgcn_readlane((int)b[1], l)});
}
__device__ __forceinline__ int expo(float d) { return (int)fmaxf(d, -4000.f); }
__device__ __forceinline__ double pow2d(float d) { return __builtin_amdgcn_ldexp(1.0, expo(d)); }
__device__ __forceinline__ u32x4 pack2(double x, double y, unsigned mx, unsigned my) {
  const u32x2 a = __builtin_bit_cast(u32x2, x), b = __builtin_bit_cast(u32x2, y);
  return u32x4{(a[0] & ~1u) | mx, a[1], (b[0] & ~1u) | my, b[1]};
}
__device__ __forceinline__ double mkd(unsigned lo, unsigned hi) { return __builtin_bit_cast(double, u32x2{lo, hi}); }
__device__ __forceinline__ void lds_barrier() {
  asm volatile("s_waitcnt lgkmcnt(0)" ::: "memory");
  __builtin_amdgcn_s_barrier();
  asm volatile("" ::: "memory");
}
template <int N> struct IC { constexpr int operator()() const { return N; } };
#define PIN() __builtin_amdgcn_sched_barrier(0)
#define RLX_WG __ATOMIC_RELAXED, __HIP_MEMORY_SCOPE_WORKGROUP

__global__ void __launch_bounds__(NTHR, 1) attn_decode(const u32x4* __restrict__ q, const char* __restrict__ k,
                                                       const char* __restrict__ v, char* __restrict__ ws,
                                                       unsigned short* __restrict__ out) {
  __shared__ __attribute__((aligned(16))) float pw[REG][20];
  __shared__ __attribute__((aligned(16))) f64x2 rec[NWAVE][4][64];
  __shared__ __attribute__((aligned(16))) double lrec[2][NWAVE];
  __shared__ __attribute__((aligned(16))) float mrec[2][NWAVE];
  __shared__ int tick[NTICK];
  __shared__ unsigned kcnt[64];
  __shared__ unsigned head[64];
  __shared__ unsigned fin[64];
  __shared__ unsigned mark[64];
  __shared__ unsigned kend[64];
  __shared__ __attribute__((aligned(16))) float rk[2][NWAVE];
  __shared__ unsigned avail;
  asm volatile("" :: "s"(q), "s"(k), "s"(v), "s"(ws), "s"(out));
  const int tid = threadIdx.x, lane = tid & 63, wave = __builtin_amdgcn_readfirstlane(tid >> 6);
  const int grp = blockIdx.x / NSPLIT, split = blockIdx.x % NSPLIT;
  char* const gws = ws + (size_t)grp * GRPWS;
  const auto rw = __builtin_amdgcn_make_buffer_rsrc((void*)gws, 0, GRPWS, 0x00020000);
  const char* kg = k + (size_t)grp * SK * D * 2;
  const char* vg = v + (size_t)grp * SK * D * 2;

  const unsigned pub = ((lane >> 5) * 4 * NSPLIT + split) * 512 + (lane & 31) * 16;
  const unsigned pubml = (lane & 31) == 0 ? MLOFF + (lane >> 5) * 128 + split * 16 : OOB;

  if (wave == NWAVE) {
    const unsigned long long t0 = __builtin_amdgcn_s_memrealtime();
    const unsigned rdo = split * NSPLIT * 512 + lane * 16;
    const unsigned rml = MLOFF + (split >> 2) * 128 + ((lane & 3) * 2 + (lane >> 5)) * 16;
    __builtin_amdgcn_s_setprio(3);
    asm volatile("" ::: "v249");
    kcnt[lane] = 2 * NWAVE;
    head[lane] = 0;
    fin[lane] = 0;
    avail = 0;
    kend[lane] = 0;
    lds_barrier();
    const u32x2 e_in = __builtin_amdgcn_raw_buffer_load_b64(rw, EOFF, 0, 16);
    u32x4 ow[5], orr[5];
#pragma unroll
    for (int i = 0; i < 4; ++i) {
      ow[i] = __builtin_amdgcn_raw_buffer_load_b128(rw, pub + i * NSPLIT * 512, 0, 16);
      orr[i] = __builtin_amdgcn_raw_buffer_load_b128(rw, rdo + i * 1024, 0, 16);
    }
    ow[4] = __builtin_amdgcn_raw_buffer_load_b128(rw, pubml, 0, 16);
    orr[4] = __builtin_amdgcn_raw_buffer_load_b128(rw, rml, 0, 16);
    PIN();
    const unsigned long long epoch = (__builtin_bit_cast(unsigned long long, e_in) + 1) & 0xffffffffffull;
    unsigned mk = 0, want[10];
#pragma unroll
    for (int i = 0; i < 10; ++i) {
      mk |= (~ow[i >> 1][(i & 1) * 2] & 1u) << i;
      want[i] = ~orr[i >> 1][(i & 1) * 2] & 1u;
    }
    mark[lane] = mk;
    asm volatile("s_waitcnt vmcnt(0)" :: "v"(want[0]), "v"(want[1]), "v"(want[2]), "v"(want[3]), "v"(want[4]), "v"(want[5]),
                 "v"(want[6]), "v"(want[7]), "v"(want[8]), "v"(want[9]) : "memory");
    while (__builtin_amdgcn_s_memrealtime() - t0 < 3300 - 100) __builtin_amdgcn_s_sleep(32);
    while (__builtin_amdgcn_s_memrealtime() - t0 < 3300) __builtin_amdgcn_s_sleep(2);
    const int nk = min(__builtin_amdgcn_readfirstlane(__hip_atomic_fetch_or(&kcnt[lane], (unsigned)SHUT, RLX_WG)), NCH);
    const int nv = nk - 2 * NWAVE, own = nv + REG - 2 * nk;
#pragma unroll
    for (int i = lane; i < REG; i += 64) tick[i] = i < nv ? VPAIR + 2 * (i + 2 * NWAVE) : split * REG + 2 * nk + i - nv;
    asm volatile("" ::: "memory");
    __hip_atomic_store(&avail, own, RLX_WG);
    while (__builtin_amdgcn_s_memrealtime() - t0 < 6640 - 800) __builtin_amdgcn_s_sleep(16);
    const int h1 = __builtin_amdgcn_readfirstlane(__hip_atomic_load(&head[0], RLX_WG));
    const unsigned t1 = (unsigned)__builtin_amdgcn_s_memrealtime();
    while (__builtin_amdgcn_s_memrealtime() - t0 < 6640 - 100) __builtin_amdgcn_s_sleep(32);
    while (__builtin_amdgcn_s_memrealtime() - t0 < 6640) __builtin_amdgcn_s_sleep(2);
    const int h2 = __builtin_amdgcn_readfirstlane(__hip_atomic_fetch_or(&head[lane], (unsigned)SHUT, RLX_WG));
    const int used = min(max(h2 + 8, nv), own);
    if (h2 <= own) __hip_atomic_store(&avail, SHUT + used, RLX_WG);
    {
      const unsigned t2 = (unsigned)__builtin_amdgcn_s_memrealtime();
      const float c = fminf((float)(t2 - t1) * __builtin_amdgcn_rcpf((float)(h2 - h1)), 2.f * 28);
      const unsigned pay = ((t2 + min(max((int)((float)(used - h2 + 6) * c), 150), 8000)) & 0x7fffu) << 9 | (2 * nk + used - nv);
      __builtin_amdgcn_raw_buffer_store_b64(__builtin_bit_cast(u32x2, epoch << 24 | pay), rw, lane == 0 ? FLOFF + split * 8 : OOB, 0, 16);
    }
    unsigned long long fl;
    for (int spin = 0; spin < (1 << 20); ++spin) {
      fl = __builtin_bit_cast(unsigned long long, __builtin_amdgcn_raw_buffer_load_b64(rw, FLOFF + (lane & 7) * 8, 0, 16));
      if (__all(fl >> 24 == epoch)) break;
      asm volatile("" ::: "memory");
    }
    const unsigned f = (unsigned)fl;
    __builtin_amdgcn_raw_buffer_store_b64(__builtin_bit_cast(u32x2, epoch), rw, split == 0 && lane == 0 ? EOFF : OOB, 0, 16);
    int nt = used;
    {
      int left[NSPLIT], ndyn = 0;
#pragma unroll
      for (int j = 0; j < NSPLIT; ++j) { left[j] = ndyn; ndyn += REG - min(__builtin_amdgcn_readlane((int)f, j) & 511, REG); }
      const int gj = (int)(((unsigned)f >> 9) - (__builtin_amdgcn_readfirstlane((unsigned)f) >> 9)) << 17 >> 17;
      const float g = (float)gj, gi = (float)__builtin_amdgcn_ds_bpermute((lane >> 3) * 4, gj);
      auto sum8 = [](float x) __attribute__((always_inline)) { x += dpp<XOR1>(x); x += dpp<XOR2>(x); return x + dpp<XOR7>(x); };
      const float lev = -key_max(-((float)(ndyn * 28) + sum8(g <= gi ? g : 0.f)) / sum8(g <= gi ? 1.f : 0.f));
      const float cut = sum8((lane & 7) < (lane >> 3) ? fmaxf(lev - g, 0.f) : 0.f) * (1.f / 28);
      const int pc = min((int)(cut + 0.5f), ndyn);
      const int lo = __builtin_amdgcn_readlane(pc, split * 8), hi = split + 1 < NSPLIT ? __builtin_amdgcn_readlane(pc, (split + 1) % NSPLIT * 8) : ndyn;
#pragma unroll 1
      for (int x0 = lo; x0 < hi + 3 * NWAVE; x0 += 64, nt += 64) {
        const int x = x0 + lane;
        int u = x + REG - left[1];
#pragma unroll
        for (int j = 1; j < NSPLIT; ++j) u = x >= left[j] ? x + (j + 1) * REG - (j + 1 < NSPLIT ? left[j + 1 < NSPLIT ? j + 1 : 0] : ndyn) : u;
        tick[nt + lane] = x < hi ? u : -1;
      }
      nt = used + hi - lo;
      asm volatile("" ::: "memory");
      __hip_atomic_store(&avail, 0x7fffffffu, RLX_WG);
    }
    const auto ro = __builtin_amdgcn_make_buffer_rsrc((void*)out, 0, NGRP * 2 * D * 2, 0x00020000);
    unsigned dst = ((grp * 2 + (split >> 2)) * D + (lane & 31) * 8 + (split & 3) * 2 + (lane >> 5)) * 2;
    asm volatile("" : "+v"(dst));
    while ((int)(__builtin_amdgcn_readfirstlane(__hip_atomic_load(&head[0], RLX_WG)) % SHUT) < nt + 5) __builtin_amdgcn_s_sleep(2);

    u32x4 in[5];
    for (int spin = 0; spin < (1 << 20); ++spin) {
      unsigned bad = 0;
#pragma unroll
      for (int i = 0; i < 5; ++i) in[i] = __builtin_amdgcn_raw_buffer_load_b128(rw, i < 4 ? rdo + i * 1024 : rml, 0, 16);
#pragma unroll
      for (int i = 0; i < 10; ++i) bad |= in[i >> 1][(i & 1) * 2] ^= want[i];
      if (__all((bad & 1u) == 0)) break;
      asm volatile("" ::: "memory");
    }
    const float mp = (float)mkd(in[4][2], in[4][3]);
    float Mx = fmaxf(mp, dpp<XOR1>(mp));
    Mx = fmaxf(Mx, dpp<XOR2>(Mx));
    const auto mh = __builtin_amdgcn_permlane32_swap(__float_as_uint(Mx), __float_as_uint(Mx), false, false);
    const int ep = expo(mp - fmaxf(__uint_as_float(mh[0]), __uint_as_float(mh[1])));
    double ls = __builtin_amdgcn_ldexp(mkd(in[4][0], in[4][1]), ep);
    ls += dpp<XOR1>(ls);
    ls += dpp<XOR2>(ls);
    ls = swap32_sum(ls, ls);
    double a0, a1;
    auto add = [&](auto n) __attribute__((always_inline)) {
      const int e = __builtin_amdgcn_mov_dpp(ep, QUAD * n(), 0xf, 0xf, true);
      const double x = __builtin_amdgcn_ldexp(mkd(in[n()][0], in[n()][1]), e), y = __builtin_amdgcn_ldexp(mkd(in[n()][2], in[n()][3]), e);
      a0 = n() ? a0 + x : x;
      a1 = n() ? a1 + y : y;
    };
    add(IC<0>{}); add(IC<1>{}); add(IC<2>{}); add(IC<3>{});
    const float rl = __builtin_amdgcn_rcpf((float)__builtin_amdgcn_frexp_mant(ls));
    const __bf16 res = (__bf16)((float)__builtin_amdgcn_ldexp(swap32_sum(a0, a1), -__builtin_amdgcn_frexp_exp(ls)) * rl);
    __builtin_amdgcn_raw_buffer_store_b16(__builtin_bit_cast(unsigned short, res), ro, dst, 0, 16);
    return;
  }

  const int r = lane >> 3, c = lane & 7;
  const unsigned kvo = r * 512 + c * 16, vvo = (lane >> 5) * 2048 + (lane & 31) * 16;
  u32x4 buf[2][4], qf[2][4];
  struct Req { __amdgpu_buffer_rsrc_t rs; unsigned vo; int so, st; };
  auto req = [&](bool isk, int unit) __attribute__((always_inline)) {
    return Req{__builtin_amdgcn_make_buffer_rsrc((void*)(isk ? kg : vg), 0, unit >= 0 ? SK * D * 2 : 0, 0x00020000),
               isk ? kvo : vvo, (unit >= 0 ? unit : 0) * UBYTES, isk ? 128 : 512};
  };
  auto fetch = [&](const Req& n, int j) __attribute__((always_inline)) {
    return __builtin_amdgcn_raw_buffer_load_b128(n.rs, n.vo, n.so + j * n.st, 2);
  };
  auto fetchv = [&](const Req& n, int j) __attribute__((always_inline)) {
    return __builtin_amdgcn_raw_buffer_load_b128(n.rs, n.vo, n.so + j * n.st, 2);
  };
  const int u0 = split * REG;
  {
    const Req a = req(true, u0 + 2 * wave), b = req(true, u0 + 2 * wave + 1);
#pragma unroll
    for (int j = 0; j < 4; ++j) { buf[0][j] = fetch(a, j); PIN(); }
#pragma unroll
    for (int g = 0; g < 2; ++g)
#pragma unroll
      for (int i = 0; i < 4; ++i) { qf[g][i] = q[(grp * 2 + g) * 32 + 8 * i + c]; PIN(); }
#pragma unroll
    for (int j = 0; j < 4; ++j) { buf[1][j] = fetch(b, j); PIN(); }
  }
  const unsigned long long t0 = __builtin_amdgcn_s_memrealtime();
  PIN();
  lds_barrier();
  PIN();

  auto scores = [&](u32x4 (&b)[4], const Req& n) __attribute__((always_inline)) {
    float d0 = 0.f, d1 = 0.f;
    asm volatile("" : "+v"(d0), "+v"(d1));
#pragma unroll
    for (int i = 0; i < 4; ++i) {
      const u32x4 kk = b[i];
#pragma unroll
      for (int e = 0; e < 4; ++e) { d0 = dot2(kk[e], qf[0][i][e], d0); d1 = dot2(kk[e], qf[1][i][e], d1); }
      asm volatile("" : "+v"(d0), "+v"(d1));
      PIN();
      b[i] = fetch(n, i);
      PIN();
    }
    asm volatile("" : "+v"(d0), "+v"(d1));
    const float t1 = d0 + dpp<XOR7>(d0), t2 = d1 + dpp<XOR7>(d1);
    float s = (c & 4) ? t2 : t1;
    s += dpp<XOR2>(s);
    return (s + dpp<XOR1>(s)) * C2;
  };
  float R = -INFINITY;
  double L = 0.0, A[8];
#pragma unroll
  for (int i = 0; i < 8; ++i) A[i] = 0.0;
  float bm;
  auto weigh = [&](float s, bool sums) __attribute__((always_inline)) {
    bm = __builtin_ceilf(key_max(s));
    const float p = __builtin_amdgcn_exp2f(s - ((bm == -INFINITY) ? 0.f : bm));
    const float Rn = fmaxf(R, 512.f * __builtin_rintf(bm * (1.f / 512.f)));
    if (__any(Rn != R)) {
      const double wa = pow2d(R - Rn), w0 = rdlane(wa, 0), w1 = rdlane(wa, 4), wh = lane < 32 ? w0 : w1;
      L *= wa;
      if (sums)
#pragma unroll
        for (int i = 0; i < 8; ++i) A[i] *= wh;
      R = Rn;
    }
    L += __builtin_amdgcn_ldexp((double)key_sum(p), expo(bm - R));
    return p;
  };
  unsigned tki = 0, tka = 0;
  int tkv = 0;
  auto values = [&](u32x4 (&b)[4], const Req& n, auto wts, int e, bool draw) __attribute__((always_inline)) {
    f32x2 acc[2][4];
    auto pv = [&](int j, f32x2 p) __attribute__((always_inline)) {
      const u32x4 vv = b[j];
#pragma unroll
      for (int e = 0; e < 4; ++e) {
        const f32x2 vf = f32x2{__builtin_bit_cast(float, vv[e] << 16), __builtin_bit_cast(float, vv[e] & 0xffff0000u)};
        if (j == 0) { acc[0][e] = vf * p[0]; acc[1][e] = vf * p[1]; }
        else {
          acc[0][e] = __builtin_elementwise_fma(vf, f32x2{p[0], p[0]}, acc[0][e]);
          acc[1][e] = __builtin_elementwise_fma(vf, f32x2{p[1], p[1]}, acc[1][e]);
        }
      }
      asm volatile("" : "+v"(acc[0][0]), "+v"(acc[0][1]), "+v"(acc[0][2]), "+v"(acc[0][3]), "+v"(acc[1][0]), "+v"(acc[1][1]), "+v"(acc[1][2]), "+v"(acc[1][3]));
      PIN();
      b[j] = fetchv(n, j);
      PIN();
    };
    pv(0, wts(IC<0>{}));
    pv(1, wts(IC<1>{}));
    if (draw) tki = __hip_atomic_fetch_add(&head[lane], 1u, RLX_WG);
    PIN();
    pv(2, wts(IC<2>{}));
    if (draw) { tki = __builtin_amdgcn_readfirstlane(tki); tka = __hip_atomic_load(&avail, RLX_WG); tkv = __hip_atomic_load(&tick[tki % NTICK], RLX_WG); }
    PIN();
    pv(3, wts(IC<3>{}));
    const double w = __builtin_amdgcn_ldexp(1.0, e);
#pragma unroll
    for (int e = 0; e < 4; ++e) {
      const f32x2 t = swap32_sum(acc[0][e], acc[1][e]);
      A[2 * e] = __builtin_fma((double)t[0], w, A[2 * e]);
      A[2 * e + 1] = __builtin_fma((double)t[1], w, A[2 * e + 1]);
    }
    asm volatile("" : "+v"(A[0]), "+v"(A[1]), "+v"(A[2]), "+v"(A[3]), "+v"(A[4]), "+v"(A[5]), "+v"(A[6]), "+v"(A[7]));
  };
  auto ticket = [&]() __attribute__((always_inline)) {
    while (tki >= __builtin_amdgcn_readfirstlane(tka)) {
      __builtin_amdgcn_s_sleep(1);
      tka = __hip_atomic_load(&avail, RLX_WG);
      tkv = __hip_atomic_load(&tick[tki % NTICK], RLX_WG);
    }
    return __builtin_amdgcn_readfirstlane(tkv);
  };

  auto clock16 = [&]() __attribute__((always_inline)) { return (unsigned)__builtin_amdgcn_s_memrealtime() << 4; };
  unsigned due = ((unsigned)t0 << 4) + 140 * 16 - (NWAVE - wave) * 440, now = due;
  auto pace = [&](int tau) __attribute__((always_inline)) {
    while ((int)(now - due) < 0) {
      now = clock16();
      if ((int)(now - due) >= 0) break;
      __builtin_amdgcn_s_sleep(1);
    }
    due += NWAVE * tau;
  };
  {
    int cur = wave;
    unsigned nxt = NWAVE + wave;
#pragma unroll 1
    for (;;) {
      pace(440);
      const bool isk = nxt < NCH;
      const int nu = u0 + 2 * (isk ? (int)nxt : wave);
      const Req a = req(isk, nu), b = req(isk, nu + 1);
      PIN();
      float* const dst = &pw[2 * cur][(c & 3) ? 16 + (c >> 2) : 2 * r + (c >> 2)];
      const float pa = weigh(scores(buf[0], a), false);
      dst[0] = (c & 3) ? bm : pa;
      const unsigned t = __hip_atomic_fetch_add(&kcnt[lane], 1u, RLX_WG);
      now = clock16();
      asm volatile("" : "+v"(L), "+v"(R));
      PIN();
      const float pb = weigh(scores(buf[1], b), false);
      dst[20] = (c & 3) ? bm : pb;
      if (!isk) break;
      cur = nxt;
      nxt = __builtin_amdgcn_readfirstlane(t);
    }
  }
  rk[(lane >> 2) & 1][wave] = R;
  asm volatile("" ::: "memory");
  __hip_atomic_fetch_add(&kend[lane], 1u, RLX_WG);
  int nxt = VPAIR + 2 * (NWAVE + wave);
  {
    struct Wt { f32x4 p[2]; float m; };
    auto wload = [&](int u) __attribute__((always_inline)) {
      const f32x4* s = (const f32x4*)&pw[u][(lane >> 5) * 8];
      return Wt{{s[0], s[1]}, pw[u][16 + (lane >> 5)]};
    };
    const float R0 = __builtin_bit_cast(float, __builtin_amdgcn_readlane(__builtin_bit_cast(int, R), 0)),
                R1 = __builtin_bit_cast(float, __builtin_amdgcn_readlane(__builtin_bit_cast(int, R), 4));
    float Rh = lane < 32 ? R0 : R1;
    Wt wa = wload(2 * wave);
    int pc = 2 * wave;
#pragma unroll 1
    for (int j = 1;; ++j) {
      pace(432);
      if (j > 1) nxt = ticket();
      const bool more = nxt >= VPAIR;
      const int pj = more ? nxt - VPAIR : 0;
      const Req a = req(!more, more ? u0 + pj : nxt), b = req(false, more ? u0 + pj + 1 : nxt);
      const Wt wb = wload(pc + 1);
      PIN();
      values(buf[0], a, [&](auto n) __attribute__((always_inline)) { return f32x2{wa.p[n() >> 1][(n() & 1) * 2], wa.p[n() >> 1][(n() & 1) * 2 + 1]}; },
             expo(wa.m - Rh), false);
      if (j == 2) {
        while (__builtin_amdgcn_readfirstlane(__hip_atomic_load(&kend[0], RLX_WG)) < NWAVE) __builtin_amdgcn_s_sleep(1);
        asm volatile("" ::: "memory");
        float Ra = R, Rb = Rh;
#pragma unroll
        for (int w = 0; w < NWAVE; ++w) { Ra = fmaxf(Ra, rk[(lane >> 2) & 1][w]); Rb = fmaxf(Rb, rk[lane >> 5][w]); }
        L *= pow2d(R - Ra);
        const double wh = pow2d(Rh - Rb);
#pragma unroll
        for (int i = 0; i < 8; ++i) A[i] *= wh;
        R = Ra;
        Rh = Rb;
      }
      wa = wload(pj);
      now = clock16();
      PIN();
      values(buf[1], b, [&](auto n) __attribute__((always_inline)) { return f32x2{wb.p[n() >> 1][(n() & 1) * 2], wb.p[n() >> 1][(n() & 1) * 2 + 1]}; },
             expo(wb.m - Rh), true);
      pc = pj;
      if (!more) break;
    }
  }
#pragma unroll 1
  while (nxt >= 0) {
    nxt = ticket();
    const Req a = req(true, nxt), b = req(false, nxt);
    PIN();
    const float p = weigh(scores(buf[0], a), true);
    const auto pr = __builtin_amdgcn_permlane16_swap(__float_as_uint(p), __float_as_uint(p), false, false);
    const int e = expo(bm - R), e0 = __builtin_amdgcn_readlane(e, 0), e1 = __builtin_amdgcn_readlane(e, 4);
    values(buf[1], b, [&](auto n) __attribute__((always_inline)) {
             const float x = __uint_as_float(pr[n() >> 1]);
             return f32x2{dpp<BCAST + (n() & 1) * 8>(x), dpp<BCAST + (n() & 1) * 8 + 4>(x)}; },
           lane < 32 ? e0 : e1, true);
  }

  {
    const unsigned mk = mark[lane];
    if (lane == 0 || lane == 4) { mrec[lane >> 2][wave] = R; lrec[lane >> 2][wave] = L; }
#pragma unroll
    for (int i = 0; i < 4; ++i) rec[wave][i][lane] = f64x2{A[2 * i], A[2 * i + 1]};
    asm volatile("" ::: "memory");
    const unsigned nth = __hip_atomic_fetch_add(&fin[lane], 1u, RLX_WG);
    asm volatile("" ::: "memory");
    float mm[NWAVE];
    double ll[NWAVE];
    f64x2 ov[NWAVE][4];
#pragma unroll
    for (int w = 0; w < NWAVE; ++w) { mm[w] = mrec[lane >> 5][w]; ll[w] = lrec[lane >> 5][w]; }
#pragma unroll
    for (int i = 0; i < 4; ++i)
#pragma unroll
      for (int w = 0; w < NWAVE; ++w) ov[w][i] = rec[w][i][lane];
    unsigned mb[10];
#pragma unroll
    for (int i = 0; i < 10; ++i) mb[i] = (mk >> i) & 1u;
    PIN();
    if (__builtin_amdgcn_readfirstlane(nth) == NWAVE - 1) {
      float Mw = mm[0];
#pragma unroll
      for (int w = 1; w < NWAVE; ++w) Mw = fmaxf(Mw, mm[w]);
      const float Ms = (Mw == -INFINITY) ? 0.f : Mw;
      double wt[NWAVE], ls = 0.0;
#pragma unroll
      for (int w = 0; w < NWAVE; ++w) { wt[w] = pow2d(mm[w] - Ms); ls = __builtin_fma(ll[w], wt[w], ls); }
      __builtin_amdgcn_raw_buffer_store_b128(pack2(ls, (double)Mw, mb[8], mb[9]), rw, pubml, 0, 16);
#pragma unroll
      for (int i = 0; i < 4; ++i) {
        double a0 = 0.0, a1 = 0.0;
#pragma unroll
        for (int w = 0; w < NWAVE; ++w) { a0 = __builtin_fma(ov[w][i][0], wt[w], a0); a1 = __builtin_fma(ov[w][i][1], wt[w], a1); }
        __builtin_amdgcn_raw_buffer_store_b128(pack2(a0, a1, mb[2 * i], mb[2 * i + 1]), rw, pub + i * NSPLIT * 512, 0, 16);
        PIN();
      }
    }
  }
  asm volatile("" :: "v"(buf[0][0]), "v"(buf[0][1]), "v"(buf[0][2]), "v"(buf[0][3]), "v"(buf[1][0]), "v"(buf[1][1]), "v"(buf[1][2]), "v"(buf[1][3]));
}

extern "C" void kernel_launch(void* const* d_in, const int* in_sizes, int n_in, void* d_out, int out_size, void* d_ws, size_t ws_size,
                              hipStream_t stream) {
  attn_decode<<<NGRP * NSPLIT, NTHR, 0, stream>>>((const u32x4*)d_in[0], (const char*)d_in[1], (const char*)d_in[2], (char*)d_ws,
                                                  (unsigned short*)d_out);
}
```
